```python
import torch

def _softmax(x, axis=-1):
    return torch.softmax(x, dim=axis)
def _gelu(x):
    return torch.nn.functional.gelu(x, approximate="tanh")
def _silu(x):
    return torch.nn.functional.silu(x)
def _sigmoid(x):
    return torch.sigmoid(x)
def _layernorm(x, axis=-1, eps=1e-5):
    m = x.mean(dim=axis, keepdim=True)
    v = x.var(dim=axis, keepdim=True, unbiased=False)
    return (x - m) / torch.sqrt(v + eps)
def _scatter_add(tgt, idx, src, axis=0):
    return tgt.clone().index_add_(axis, idx, src)
def _segment_sum(src, seg, n):
    out = torch.zeros((n,) + src.shape[1:], dtype=src.dtype)
    return out.index_add_(0, seg, src)
def _pad(x, pw):
    flat = tuple(v for pair in reversed(pw) for v in pair)
    return torch.nn.functional.pad(x, flat)

kernel_name = 'gated_mix__softmax_full_mha__mixer_token_mlp__prompt'

def setup_inputs():

    _w = (lambda a: (lambda t: t.to(torch.bfloat16) if t.is_floating_point() else t)(torch.as_tensor(a)))
    def _A_setup():
        B, S, D = 1, 2048, 512
        return {
            "x": _w((lambda *s: torch.randn(*s, dtype=torch.bfloat16) * 0.05)(B, S, D)),
            "wqkv": _w((lambda *s: torch.randn(*s, dtype=torch.bfloat16) * 0.8)(D, 3 * D)),
            "wo": _w((lambda *s: torch.randn(*s, dtype=torch.bfloat16) * 0.05)(D, D)),
        }
    _A_in = _A_setup()

    def _B_setup():
        B, S, D, F = 1, 2048, 512, 1792
        return {
            "x": _w((lambda *s: torch.randn(*s, dtype=torch.bfloat16) * 0.05)(B, S, D)),
            "w1": _w((lambda *s: torch.randn(*s, dtype=torch.bfloat16) * 0.8)(S, F) * (S ** -0.5)),
            "b1": _w((lambda *s: torch.randn(*s, dtype=torch.bfloat16) * 12.8)(F) * 0.02),
            "w2": _w((lambda *s: torch.randn(*s, dtype=torch.bfloat16) * 0.05)(F, S) * (F ** -0.5)),
            "b2": _w((lambda *s: torch.randn(*s, dtype=torch.bfloat16) * 0.8)(S) * 0.02),
        }
    _B_in = _B_setup()

    return {
        "A_x": _A_in["x"],
        "A_wqkv": _A_in["wqkv"],
        "A_wo": _A_in["wo"],
        "B_x": _B_in["x"],
        "B_w1": _B_in["w1"],
        "B_b1": _B_in["b1"],
        "B_w2": _B_in["w2"],
        "B_b2": _B_in["b2"],
        "gate_w": _w((lambda *s: torch.randn(*s, dtype=torch.bfloat16) * 0.05)(512) * 16.0),
    }

def reference(A_x, A_wqkv, A_wo, B_x, B_w1, B_b1, B_w2, B_b2, gate_w):
    def _A_ref(x, wqkv, wo):
        B, S, D = x.shape
        H = 32; Dh = D // H
        qkv = (lambda x, *d: x.permute(*d))((x @ wqkv).reshape(B, S, 3, H, Dh), 2, 0, 3, 1, 4)
        q, k, v = qkv[0], qkv[1], qkv[2]
        scale = Dh ** -0.5
        scores = torch.einsum("bhqd,bhkd->bhqk", q, k) * scale
        p = _softmax(scores, axis=-1)
        o = torch.einsum("bhqk,bhkd->bhqd", p, v)
        return (lambda x, *d: x.permute(*d))(o, 0, 2, 1, 3).reshape(B, S, D) @ wo

    def _B_ref(x, w1, b1, w2, b2):
        B, S, D = x.shape
        h = _layernorm(x, axis=-1)
        u = torch.einsum("bsd,sf->bdf", h, w1) + b1
        u = _gelu(u)
        y = torch.einsum("bdf,fs->bds", u, w2) + b2
        return (lambda x, *d: x.permute(*d))(y, 0, 2, 1)

    x = A_x
    g = _sigmoid((x * gate_w).sum(-1, keepdims=True))
    a = _A_ref(x, A_wqkv, A_wo)
    b = _B_ref(x, B_w1, B_b1, B_w2, B_b2)
    return g * a + (1 - g) * b
```

```cpp
#include <hip/hip_runtime.h>
#include <hip/hip_bf16.h>
typedef __hip_bfloat16 bf16;
static constexpr int S = 2048, D = 512, H = 32, DH = 16, F = 1792;
__device__ inline float b2f(bf16 v) { return __bfloat162float(v); }

template <typename TA, bool TRANS_A>
__global__ void gemm_naive(const TA* A, const bf16* B, float* C, int M, int N, int K) {
  int n = blockIdx.x * 64 + (threadIdx.x & 63);
  int m = blockIdx.y * 4 + (threadIdx.x >> 6);
  float acc = 0.f;
  for (int k = 0; k < K; ++k) {
    float a = TRANS_A ? (float)A[(long)k * M + m] : (float)A[(long)m * K + k];
    acc += a * b2f(B[(long)k * N + n]);
  }
  C[(long)m * N + n] = acc;
}
__global__ void attn_naive(const float* qkv, float* o) {
  int h = blockIdx.y, q = blockIdx.x * 64 + threadIdx.x;
  float qv[DH];
  for (int d = 0; d < DH; ++d) qv[d] = qkv[(long)q * 3 * D + h * DH + d] * 0.25f;
  float m = -1e30f, l = 0.f, acc[DH];
  for (int d = 0; d < DH; ++d) acc[d] = 0.f;
  for (int k = 0; k < S; ++k) {
    const float* kp = qkv + (long)k * 3 * D + D + h * DH;
    const float* vp = qkv + (long)k * 3 * D + 2 * D + h * DH;
    float s = 0.f;
    for (int d = 0; d < DH; ++d) s += qv[d] * kp[d];
    float mn = fmaxf(m, s), al = __expf(m - mn), p = __expf(s - mn);
    l = l * al + p;
    for (int d = 0; d < DH; ++d) acc[d] = acc[d] * al + p * vp[d];
    m = mn;
  }
  for (int d = 0; d < DH; ++d) o[(long)q * D + h * DH + d] = acc[d] / l;
}
__global__ void ln_gate(const bf16* bx, const bf16* ax, const bf16* gw, float* hh, float* g) {
  int s = blockIdx.x, t = threadIdx.x;
  float v[8], sum = 0.f, gs = 0.f;
  for (int i = 0; i < 8; ++i) { v[i] = b2f(bx[s * D + t * 8 + i]); sum += v[i]; gs += b2f(ax[s * D + t * 8 + i]) * b2f(gw[t * 8 + i]); }
  for (int o = 32; o; o >>= 1) { sum += __shfl_xor(sum, o); gs += __shfl_xor(gs, o); }
  float mean = sum / D, var = 0.f;
  for (int i = 0; i < 8; ++i) { float d = v[i] - mean; var += d * d; }
  for (int o = 32; o; o >>= 1) var += __shfl_xor(var, o);
  float r = 1.f / sqrtf(var / D + 1e-5f);
  for (int i = 0; i < 8; ++i) hh[s * D + t * 8 + i] = (v[i] - mean) * r;
  if (t == 0) g[s] = 1.f / (1.f + __expf(-gs));
}
__global__ void gelu_bias(float* u, const bf16* b1) {
  int i = blockIdx.x * 256 + threadIdx.x;
  float x = u[i] + b2f(b1[i % F]);
  float y = 0.7978845608028654f * (x + 0.044715f * x * x * x);
  u[i] = 0.5f * x * (1.f + tanhf(y));
}
__global__ void combine(const float* a, const float* y, const bf16* b2, const float* g, bf16* out) {
  int i = blockIdx.x * 256 + threadIdx.x;
  int s = i / D, d = i % D;
  float b = y[(long)d * S + s] + b2f(b2[s]);
  out[i] = __float2bfloat16(g[s] * a[i] + (1.f - g[s]) * b);
}
extern "C" void kernel_launch(void* const* d_in, const int* in_sizes, int n_in, void* d_out, int out_size, void* d_ws, size_t ws_size, hipStream_t stream) {
  const bf16* ax = (const bf16*)d_in[0]; const bf16* wqkv = (const bf16*)d_in[1]; const bf16* wo = (const bf16*)d_in[2];
  const bf16* bx = (const bf16*)d_in[3]; const bf16* w1 = (const bf16*)d_in[4]; const bf16* b1 = (const bf16*)d_in[5];
  const bf16* w2 = (const bf16*)d_in[6]; const bf16* b2 = (const bf16*)d_in[7]; const bf16* gw = (const bf16*)d_in[8];
  float* ws = (float*)d_ws;
  float* qkv = ws; float* o = qkv + S * 3 * D; float* a = o + S * D; float* hh = a + S * D; float* g = hh + S * D; float* u = g + S; float* y = u + D * F;
  gemm_naive<bf16, false><<<dim3(3 * D / 64, S / 4), 256, 0, stream>>>(ax, wqkv, qkv, S, 3 * D, D);
  attn_naive<<<dim3(S / 64, H), 64, 0, stream>>>(qkv, o);
  gemm_naive<float, false><<<dim3(D / 64, S / 4), 256, 0, stream>>>(o, wo, a, S, D, D);
  ln_gate<<<S, 64, 0, stream>>>(ax, ax, gw, hh, g);
  gemm_naive<float, true><<<dim3(F / 64, D / 4), 256, 0, stream>>>(hh, w1, u, D, F, S);
  gelu_bias<<<D * F / 256, 256, 0, stream>>>(u, b1);
  gemm_naive<float, false><<<dim3(S / 64, D / 4), 256, 0, stream>>>(u, w2, y, D, S, F);
  combine<<<S * D / 256, 256, 0, stream>>>(a, y, b2, g, (bf16*)d_out);
}
```

```cpp
#include <hip/hip_runtime.h>
#include <hip/hip_bf16.h>
#include <stdint.h>

typedef __hip_bfloat16 bf16;
typedef short bf16x8 __attribute__((ext_vector_type(8)));
typedef short s16x4 __attribute__((ext_vector_type(4)));
typedef float f32x16 __attribute__((ext_vector_type(16)));
typedef float f32x4 __attribute__((ext_vector_type(4)));
typedef float f32x2 __attribute__((ext_vector_type(2)));
typedef __bf16 bf16x2_t __attribute__((ext_vector_type(2)));
typedef unsigned u32x4 __attribute__((ext_vector_type(4)));
typedef unsigned u32x2 __attribute__((ext_vector_type(2)));
typedef __attribute__((address_space(3))) char* lds_ptr;
typedef __attribute__((address_space(3))) const char* lds_cptr;
typedef short v4i16_t __attribute__((ext_vector_type(4)));
#define DI __device__ __forceinline__
#define MFMA(a, b, c) __builtin_amdgcn_mfma_f32_32x32x16_bf16((a), (b), (c), 0, 0, 0)

constexpr int S = 2048, D = 512, NH = 32, FF = 1792;
constexpr size_t WS_Q = 0;
constexpr size_t WS_K = WS_Q + (2u << 20);
constexpr size_t WS_V = WS_K + (2u << 20);
constexpr size_t WS_H = WS_V + (2u << 20);
constexpr size_t WS_U = WS_H + (2u << 20);
constexpr size_t WS_O = WS_U + (2u << 20);
constexpr size_t WS_G = WS_O + (2u << 20);
constexpr size_t WS_F2 = WS_G + 8192;
constexpr unsigned SENTINEL = 0xffffffffu;

DI unsigned rfl(unsigned x) { return __builtin_amdgcn_readfirstlane(x); }
DI unsigned cvtpk(float lo, float hi) { f32x2 v = {lo, hi}; bf16x2_t b = __builtin_convertvector(v, bf16x2_t); return __builtin_bit_cast(unsigned, b); }
DI float bflo(unsigned w) { return __uint_as_float(w << 16); }
DI float bfhi(unsigned w) { return __uint_as_float(w & 0xffff0000u); }
DI void glds16(const void* g, unsigned lds_base) {
  unsigned sv;
  asm volatile("s_mov_b32 %0, m0\n\ts_mov_b32 m0, %2\n\ts_nop 0\n\tglobal_load_lds_dwordx4 %1, off\n\ts_mov_b32 m0, %0" : "=&s"(sv) : "v"(g), "s"(lds_base) : "memory");
}
DI void glds16_sc1(const void* g, unsigned lds_base) {
  unsigned sv;
  asm volatile("s_mov_b32 %0, m0\n\ts_mov_b32 m0, %2\n\ts_nop 0\n\tglobal_load_lds_dwordx4 %1, off sc1\n\ts_mov_b32 m0, %0" : "=&s"(sv) : "v"(g), "s"(lds_base) : "memory");
}
DI void st16_sc1(void* p, u32x4 v) { asm volatile("global_store_dwordx4 %0, %1, off sc1\n\ts_nop 1" ::"v"(p), "v"(v) : "memory"); }
DI void st4_sc1(void* p, unsigned v) { asm volatile("global_store_dword %0, %1, off sc1" ::"v"(p), "v"(v) : "memory"); }
DI u32x4 ld16_sc1(const void* p) { u32x4 v; asm volatile("global_load_dwordx4 %0, %1, off sc1\n\ts_waitcnt vmcnt(0)" : "=&v"(v) : "v"(p) : "memory"); return v; }
DI unsigned ld4_sc1(const void* p) { unsigned v; asm volatile("global_load_dword %0, %1, off sc1\n\ts_waitcnt vmcnt(0)" : "=&v"(v) : "v"(p) : "memory"); return v; }
DI s16x4 vtr(lds_cptr p) { return __builtin_bit_cast(s16x4, __builtin_amdgcn_ds_read_tr16_b64_v4i16((__attribute__((address_space(3))) v4i16_t*)p)); }
DI bf16x8 cat8(s16x4 lo, s16x4 hi) { return __builtin_shufflevector(lo, hi, 0, 1, 2, 3, 4, 5, 6, 7); }
DI u32x4 pack8(const f32x16& c, int o, float sc) {
  u32x4 r;
  r[0] = cvtpk(c[o + 0] * sc, c[o + 1] * sc); r[1] = cvtpk(c[o + 2] * sc, c[o + 3] * sc);
  r[2] = cvtpk(c[o + 4] * sc, c[o + 5] * sc); r[3] = cvtpk(c[o + 6] * sc, c[o + 7] * sc);
  return r;
}


#define ATTN_ASM \
    "s_setprio 1\n" \
    "s_movk_i32 s46, 0x5f\n" \
    "s_movk_i32 s43, 0x54\n" \
    "s_mov_b32 s44, 0\n" \
    "s_mov_b32 s48, 0x0000ffff\n" \
    "s_mov_b32 s49, 0x0000ffff\n" \
    "v_mfma_f32_32x32x16_bf16 v[64:79], v[168:171], v[160:163], v[128:143]\n" \
    "v_mov_b32 v152, 0\n" \
    "v_mov_b32 v153, 0\n" \
    "v_mov_b32 v154, 0\n" \
    "v_mov_b32 v155, 0\n" \
    "v_mov_b32 v156, 0\n" \
    "v_mov_b32 v157, 0\n" \
    "v_mov_b32 v158, 0\n" \
    "v_mov_b32 v159, 0\n" \
    "v_mov_b32 v172, v184\n" \
    "v_mov_b32 v173, v184\n" \
    "v_mov_b32 v174, v184\n" \
    "v_mov_b32 v175, v184\n" \
    "v_mov_b32 v176, v184\n" \
    "v_mov_b32 v177, v184\n" \
    "v_mov_b32 v178, v184\n" \
    "v_mov_b32 v179, v184\n" \
    "v_mov_b32 v96, 0\n" \
    "v_mov_b32 v97, 0\n" \
    "v_mov_b32 v98, 0\n" \
    "v_mov_b32 v99, 0\n" \
    "v_mov_b32 v100, 0\n" \
    "v_mov_b32 v101, 0\n" \
    "v_mov_b32 v102, 0\n" \
    "v_mov_b32 v103, 0\n" \
    "v_mov_b32 v104, 0\n" \
    "v_mov_b32 v105, 0\n" \
    "v_mov_b32 v106, 0\n" \
    "v_mov_b32 v107, 0\n" \
    "v_mov_b32 v108, 0\n" \
    "v_mov_b32 v109, 0\n" \
    "v_mov_b32 v110, 0\n" \
    "v_mov_b32 v111, 0\n" \
    "v_mov_b32 v112, 0\n" \
    "v_mov_b32 v113, 0\n" \
    "v_mov_b32 v114, 0\n" \
    "v_mov_b32 v115, 0\n" \
    "v_mov_b32 v116, 0\n" \
    "v_mov_b32 v117, 0\n" \
    "v_mov_b32 v118, 0\n" \
    "v_mov_b32 v119, 0\n" \
    "v_mov_b32 v120, 0\n" \
    "v_mov_b32 v121, 0\n" \
    "v_mov_b32 v122, 0\n" \
    "v_mov_b32 v123, 0\n" \
    "v_mov_b32 v124, 0\n" \
    "v_mov_b32 v125, 0\n" \
    "v_mov_b32 v126, 0\n" \
    "v_mov_b32 v127, 0\n" \
    "L_chunk_%=:\n" \
    "v_mfma_f32_32x32x16_bf16 v[80:95], v[168:171], v[164:167], v[128:143]\n" \
    "ds_read_b128 v[168:171], v180 offset:1024\n" \
    "v_exp_f32 v64, v64\n" \
    "v_exp_f32 v65, v65\n" \
    "v_exp_f32 v66, v66\n" \
    "v_cvt_pk_bf16_f32 v144, v64, v65\n" \
    "v_exp_f32 v67, v67\n" \
    "v_exp_f32 v68, v68\n" \
    "v_cvt_pk_bf16_f32 v145, v66, v67\n" \
    "v_mfma_f32_32x32x16_bf16 v[112:127], v[172:175], v[152:155], v[112:127]\n" \
    "v_exp_f32 v69, v69\n" \
    "v_exp_f32 v70, v70\n" \
    "v_cvt_pk_bf16_f32 v146, v68, v69\n" \
    "v_exp_f32 v71, v71\n" \
    "v_exp_f32 v72, v72\n" \
    "v_cvt_pk_bf16_f32 v147, v70, v71\n" \
    "v_exp_f32 v73, v73\n" \
    "v_exp_f32 v74, v74\n" \
    "v_cvt_pk_bf16_f32 v148, v72, v73\n" \
    "v_mfma_f32_32x32x16_bf16 v[112:127], v[176:179], v[156:159], v[112:127]\n" \
    "s_mov_b64 exec, s[48:49]\n" \
    "ds_read_b128 v[172:175], v181 offset:0\n" \
    "ds_read_b128 v[176:179], v181 offset:512\n" \
    "s_mov_b64 exec, -1\n" \
    "v_exp_f32 v75, v75\n" \
    "v_exp_f32 v76, v76\n" \
    "v_cvt_pk_bf16_f32 v149, v74, v75\n" \
    "v_exp_f32 v77, v77\n" \
    "v_exp_f32 v78, v78\n" \
    "v_cvt_pk_bf16_f32 v150, v76, v77\n" \
    "v_exp_f32 v79, v79\n" \
    "s_waitcnt lgkmcnt(2)\n" \
    "v_cvt_pk_bf16_f32 v151, v78, v79\n" \
    "v_mfma_f32_32x32x16_bf16 v[64:79], v[168:171], v[160:163], v[128:143]\n" \
    "v_exp_f32 v80, v80\n" \
    "v_exp_f32 v81, v81\n" \
    "v_exp_f32 v82, v82\n" \
    "v_cvt_pk_bf16_f32 v152, v80, v81\n" \
    "v_exp_f32 v83, v83\n" \
    "v_exp_f32 v84, v84\n" \
    "v_cvt_pk_bf16_f32 v153, v82, v83\n" \
    "s_waitcnt lgkmcnt(0)\n" \
    "v_mfma_f32_32x32x16_bf16 v[96:111], v[172:175], v[144:147], v[96:111]\n" \
    "v_exp_f32 v85, v85\n" \
    "v_exp_f32 v86, v86\n" \
    "v_cvt_pk_bf16_f32 v154, v84, v85\n" \
    "v_exp_f32 v87, v87\n" \
    "v_exp_f32 v88, v88\n" \
    "v_cvt_pk_bf16_f32 v155, v86, v87\n" \
    "v_exp_f32 v89, v89\n" \
    "v_exp_f32 v90, v90\n" \
    "v_cvt_pk_bf16_f32 v156, v88, v89\n" \
    "v_mfma_f32_32x32x16_bf16 v[96:111], v[176:179], v[148:151], v[96:111]\n" \
    "v_exp_f32 v91, v91\n" \
    "v_exp_f32 v92, v92\n" \
    "v_cvt_pk_bf16_f32 v157, v90, v91\n" \
    "v_exp_f32 v93, v93\n" \
    "v_exp_f32 v94, v94\n" \
    "v_cvt_pk_bf16_f32 v158, v92, v93\n" \
    "v_exp_f32 v95, v95\n" \
    "s_nop 0\n" \
    "v_cvt_pk_bf16_f32 v159, v94, v95\n" \
    "v_mfma_f32_32x32x16_bf16 v[80:95], v[168:171], v[164:167], v[128:143]\n" \
    "ds_read_b128 v[168:171], v180 offset:2048\n" \
    "v_exp_f32 v64, v64\n" \
    "v_exp_f32 v65, v65\n" \
    "v_exp_f32 v66, v66\n" \
    "v_cvt_pk_bf16_f32 v144, v64, v65\n" \
    "v_exp_f32 v67, v67\n" \
    "v_exp_f32 v68, v68\n" \
    "v_cvt_pk_bf16_f32 v145, v66, v67\n" \
    "v_mfma_f32_32x32x16_bf16 v[112:127], v[172:175], v[152:155], v[112:127]\n" \
    "v_exp_f32 v69, v69\n" \
    "v_exp_f32 v70, v70\n" \
    "v_cvt_pk_bf16_f32 v146, v68, v69\n" \
    "v_exp_f32 v71, v71\n" \
    "v_exp_f32 v72, v72\n" \
    "v_cvt_pk_bf16_f32 v147, v70, v71\n" \
    "v_exp_f32 v73, v73\n" \
    "v_exp_f32 v74, v74\n" \
    "v_cvt_pk_bf16_f32 v148, v72, v73\n" \
    "v_mfma_f32_32x32x16_bf16 v[112:127], v[176:179], v[156:159], v[112:127]\n" \
    "s_mov_b64 exec, s[48:49]\n" \
    "ds_read_b128 v[172:175], v181 offset:1024\n" \
    "ds_read_b128 v[176:179], v181 offset:1536\n" \
    "s_mov_b64 exec, -1\n" \
    "v_exp_f32 v75, v75\n" \
    "v_exp_f32 v76, v76\n" \
    "v_cvt_pk_bf16_f32 v149, v74, v75\n" \
    "v_exp_f32 v77, v77\n" \
    "v_exp_f32 v78, v78\n" \
    "v_cvt_pk_bf16_f32 v150, v76, v77\n" \
    "v_exp_f32 v79, v79\n" \
    "s_waitcnt lgkmcnt(2)\n" \
    "v_cvt_pk_bf16_f32 v151, v78, v79\n" \
    "v_mfma_f32_32x32x16_bf16 v[64:79], v[168:171], v[160:163], v[128:143]\n" \
    "v_exp_f32 v80, v80\n" \
    "v_exp_f32 v81, v81\n" \
    "v_exp_f32 v82, v82\n" \
    "v_cvt_pk_bf16_f32 v152, v80, v81\n" \
    "v_exp_f32 v83, v83\n" \
    "v_exp_f32 v84, v84\n" \
    "v_cvt_pk_bf16_f32 v153, v82, v83\n" \
    "s_waitcnt lgkmcnt(0)\n" \
    "v_mfma_f32_32x32x16_bf16 v[96:111], v[172:175], v[144:147], v[96:111]\n" \
    "v_exp_f32 v85, v85\n" \
    "v_exp_f32 v86, v86\n" \
    "v_cvt_pk_bf16_f32 v154, v84, v85\n" \
    "v_exp_f32 v87, v87\n" \
    "v_exp_f32 v88, v88\n" \
    "v_cvt_pk_bf16_f32 v155, v86, v87\n" \
    "v_exp_f32 v89, v89\n" \
    "v_exp_f32 v90, v90\n" \
    "v_cvt_pk_bf16_f32 v156, v88, v89\n" \
    "v_mfma_f32_32x32x16_bf16 v[96:111], v[176:179], v[148:151], v[96:111]\n" \
    "v_exp_f32 v91, v91\n" \
    "v_exp_f32 v92, v92\n" \
    "v_cvt_pk_bf16_f32 v157, v90, v91\n" \
    "v_exp_f32 v93, v93\n" \
    "v_exp_f32 v94, v94\n" \
    "v_cvt_pk_bf16_f32 v158, v92, v93\n" \
    "v_exp_f32 v95, v95\n" \
    "s_nop 0\n" \
    "v_cvt_pk_bf16_f32 v159, v94, v95\n" \
    "v_mfma_f32_32x32x16_bf16 v[80:95], v[168:171], v[164:167], v[128:143]\n" \
    "ds_read_b128 v[168:171], v180 offset:3072\n" \
    "v_exp_f32 v64, v64\n" \
    "v_exp_f32 v65, v65\n" \
    "v_exp_f32 v66, v66\n" \
    "v_cvt_pk_bf16_f32 v144, v64, v65\n" \
    "v_exp_f32 v67, v67\n" \
    "v_exp_f32 v68, v68\n" \
    "v_cvt_pk_bf16_f32 v145, v66, v67\n" \
    "v_mfma_f32_32x32x16_bf16 v[112:127], v[172:175], v[152:155], v[112:127]\n" \
    "v_exp_f32 v69, v69\n" \
    "v_exp_f32 v70, v70\n" \
    "v_cvt_pk_bf16_f32 v146, v68, v69\n" \
    "v_exp_f32 v71, v71\n" \
    "v_exp_f32 v72, v72\n" \
    "v_cvt_pk_bf16_f32 v147, v70, v71\n" \
    "v_exp_f32 v73, v73\n" \
    "v_exp_f32 v74, v74\n" \
    "v_cvt_pk_bf16_f32 v148, v72, v73\n" \
    "v_mfma_f32_32x32x16_bf16 v[112:127], v[176:179], v[156:159], v[112:127]\n" \
    "s_mov_b64 exec, s[48:49]\n" \
    "ds_read_b128 v[172:175], v181 offset:2048\n" \
    "ds_read_b128 v[176:179], v181 offset:2560\n" \
    "s_mov_b64 exec, -1\n" \
    "v_exp_f32 v75, v75\n" \
    "v_exp_f32 v76, v76\n" \
    "v_cvt_pk_bf16_f32 v149, v74, v75\n" \
    "v_exp_f32 v77, v77\n" \
    "v_exp_f32 v78, v78\n" \
    "v_cvt_pk_bf16_f32 v150, v76, v77\n" \
    "v_exp_f32 v79, v79\n" \
    "s_waitcnt lgkmcnt(2)\n" \
    "v_cvt_pk_bf16_f32 v151, v78, v79\n" \
    "v_mfma_f32_32x32x16_bf16 v[64:79], v[168:171], v[160:163], v[128:143]\n" \
    "v_exp_f32 v80, v80\n" \
    "v_exp_f32 v81, v81\n" \
    "v_exp_f32 v82, v82\n" \
    "v_cvt_pk_bf16_f32 v152, v80, v81\n" \
    "v_exp_f32 v83, v83\n" \
    "v_exp_f32 v84, v84\n" \
    "v_cvt_pk_bf16_f32 v153, v82, v83\n" \
    "s_waitcnt lgkmcnt(0)\n" \
    "v_mfma_f32_32x32x16_bf16 v[96:111], v[172:175], v[144:147], v[96:111]\n" \
    "v_exp_f32 v85, v85\n" \
    "v_exp_f32 v86, v86\n" \
    "v_cvt_pk_bf16_f32 v154, v84, v85\n" \
    "v_exp_f32 v87, v87\n" \
    "v_exp_f32 v88, v88\n" \
    "v_cvt_pk_bf16_f32 v155, v86, v87\n" \
    "v_exp_f32 v89, v89\n" \
    "v_exp_f32 v90, v90\n" \
    "v_cvt_pk_bf16_f32 v156, v88, v89\n" \
    "v_mfma_f32_32x32x16_bf16 v[96:111], v[176:179], v[148:151], v[96:111]\n" \
    "v_exp_f32 v91, v91\n" \
    "v_exp_f32 v92, v92\n" \
    "v_cvt_pk_bf16_f32 v157, v90, v91\n" \
    "v_exp_f32 v93, v93\n" \
    "v_exp_f32 v94, v94\n" \
    "v_cvt_pk_bf16_f32 v158, v92, v93\n" \
    "v_exp_f32 v95, v95\n" \
    "s_nop 0\n" \
    "v_cvt_pk_bf16_f32 v159, v94, v95\n" \
    "v_mfma_f32_32x32x16_bf16 v[80:95], v[168:171], v[164:167], v[128:143]\n" \
    "ds_read_b128 v[168:171], v180 offset:4096\n" \
    "v_exp_f32 v64, v64\n" \
    "v_exp_f32 v65, v65\n" \
    "v_exp_f32 v66, v66\n" \
    "v_cvt_pk_bf16_f32 v144, v64, v65\n" \
    "v_exp_f32 v67, v67\n" \
    "v_exp_f32 v68, v68\n" \
    "v_cvt_pk_bf16_f32 v145, v66, v67\n" \
    "v_mfma_f32_32x32x16_bf16 v[112:127], v[172:175], v[152:155], v[112:127]\n" \
    "v_exp_f32 v69, v69\n" \
    "v_exp_f32 v70, v70\n" \
    "v_cvt_pk_bf16_f32 v146, v68, v69\n" \
    "v_exp_f32 v71, v71\n" \
    "v_exp_f32 v72, v72\n" \
    "v_cvt_pk_bf16_f32 v147, v70, v71\n" \
    "v_exp_f32 v73, v73\n" \
    "v_exp_f32 v74, v74\n" \
    "v_cvt_pk_bf16_f32 v148, v72, v73\n" \
    "v_mfma_f32_32x32x16_bf16 v[112:127], v[176:179], v[156:159], v[112:127]\n" \
    "s_mov_b64 exec, s[48:49]\n" \
    "ds_read_b128 v[172:175], v181 offset:3072\n" \
    "ds_read_b128 v[176:179], v181 offset:3584\n" \
    "s_mov_b64 exec, -1\n" \
    "v_exp_f32 v75, v75\n" \
    "v_exp_f32 v76, v76\n" \
    "v_cvt_pk_bf16_f32 v149, v74, v75\n" \
    "v_exp_f32 v77, v77\n" \
    "v_exp_f32 v78, v78\n" \
    "v_cvt_pk_bf16_f32 v150, v76, v77\n" \
    "v_exp_f32 v79, v79\n" \
    "s_waitcnt lgkmcnt(2)\n" \
    "v_cvt_pk_bf16_f32 v151, v78, v79\n" \
    "v_mfma_f32_32x32x16_bf16 v[64:79], v[168:171], v[160:163], v[128:143]\n" \
    "v_exp_f32 v80, v80\n" \
    "v_exp_f32 v81, v81\n" \
    "v_exp_f32 v82, v82\n" \
    "v_cvt_pk_bf16_f32 v152, v80, v81\n" \
    "v_exp_f32 v83, v83\n" \
    "v_exp_f32 v84, v84\n" \
    "v_cvt_pk_bf16_f32 v153, v82, v83\n" \
    "s_waitcnt lgkmcnt(0)\n" \
    "v_mfma_f32_32x32x16_bf16 v[96:111], v[172:175], v[144:147], v[96:111]\n" \
    "v_exp_f32 v85, v85\n" \
    "v_exp_f32 v86, v86\n" \
    "v_cvt_pk_bf16_f32 v154, v84, v85\n" \
    "v_exp_f32 v87, v87\n" \
    "v_exp_f32 v88, v88\n" \
    "v_cvt_pk_bf16_f32 v155, v86, v87\n" \
    "v_exp_f32 v89, v89\n" \
    "v_exp_f32 v90, v90\n" \
    "v_cvt_pk_bf16_f32 v156, v88, v89\n" \
    "v_mfma_f32_32x32x16_bf16 v[96:111], v[176:179], v[148:151], v[96:111]\n" \
    "v_exp_f32 v91, v91\n" \
    "v_exp_f32 v92, v92\n" \
    "v_cvt_pk_bf16_f32 v157, v90, v91\n" \
    "v_exp_f32 v93, v93\n" \
    "v_exp_f32 v94, v94\n" \
    "v_cvt_pk_bf16_f32 v158, v92, v93\n" \
    "v_exp_f32 v95, v95\n" \
    "s_nop 0\n" \
    "v_cvt_pk_bf16_f32 v159, v94, v95\n" \
    "v_mfma_f32_32x32x16_bf16 v[80:95], v[168:171], v[164:167], v[128:143]\n" \
    "ds_read_b128 v[168:171], v180 offset:5120\n" \
    "v_exp_f32 v64, v64\n" \
    "v_exp_f32 v65, v65\n" \
    "v_exp_f32 v66, v66\n" \
    "v_cvt_pk_bf16_f32 v144, v64, v65\n" \
    "v_exp_f32 v67, v67\n" \
    "v_exp_f32 v68, v68\n" \
    "v_cvt_pk_bf16_f32 v145, v66, v67\n" \
    "v_mfma_f32_32x32x16_bf16 v[112:127], v[172:175], v[152:155], v[112:127]\n" \
    "v_exp_f32 v69, v69\n" \
    "v_exp_f32 v70, v70\n" \
    "v_cvt_pk_bf16_f32 v146, v68, v69\n" \
    "v_exp_f32 v71, v71\n" \
    "v_exp_f32 v72, v72\n" \
    "v_cvt_pk_bf16_f32 v147, v70, v71\n" \
    "v_exp_f32 v73, v73\n" \
    "v_exp_f32 v74, v74\n" \
    "v_cvt_pk_bf16_f32 v148, v72, v73\n" \
    "v_mfma_f32_32x32x16_bf16 v[112:127], v[176:179], v[156:159], v[112:127]\n" \
    "s_mov_b64 exec, s[48:49]\n" \
    "ds_read_b128 v[172:175], v181 offset:4096\n" \
    "ds_read_b128 v[176:179], v181 offset:4608\n" \
    "s_mov_b64 exec, -1\n" \
    "v_exp_f32 v75, v75\n" \
    "v_exp_f32 v76, v76\n" \
    "v_cvt_pk_bf16_f32 v149, v74, v75\n" \
    "v_exp_f32 v77, v77\n" \
    "v_exp_f32 v78, v78\n" \
    "v_cvt_pk_bf16_f32 v150, v76, v77\n" \
    "v_exp_f32 v79, v79\n" \
    "s_waitcnt lgkmcnt(2)\n" \
    "v_cvt_pk_bf16_f32 v151, v78, v79\n" \
    "v_mfma_f32_32x32x16_bf16 v[64:79], v[168:171], v[160:163], v[128:143]\n" \
    "v_exp_f32 v80, v80\n" \
    "v_exp_f32 v81, v81\n" \
    "v_exp_f32 v82, v82\n" \
    "v_cvt_pk_bf16_f32 v152, v80, v81\n" \
    "v_exp_f32 v83, v83\n" \
    "v_exp_f32 v84, v84\n" \
    "v_cvt_pk_bf16_f32 v153, v82, v83\n" \
    "s_waitcnt lgkmcnt(0)\n" \
    "v_mfma_f32_32x32x16_bf16 v[96:111], v[172:175], v[144:147], v[96:111]\n" \
    "v_exp_f32 v85, v85\n" \
    "v_exp_f32 v86, v86\n" \
    "v_cvt_pk_bf16_f32 v154, v84, v85\n" \
    "v_exp_f32 v87, v87\n" \
    "v_exp_f32 v88, v88\n" \
    "v_cvt_pk_bf16_f32 v155, v86, v87\n" \
    "v_exp_f32 v89, v89\n" \
    "v_exp_f32 v90, v90\n" \
    "v_cvt_pk_bf16_f32 v156, v88, v89\n" \
    "v_mfma_f32_32x32x16_bf16 v[96:111], v[176:179], v[148:151], v[96:111]\n" \
    "v_exp_f32 v91, v91\n" \
    "v_exp_f32 v92, v92\n" \
    "v_cvt_pk_bf16_f32 v157, v90, v91\n" \
    "v_exp_f32 v93, v93\n" \
    "v_exp_f32 v94, v94\n" \
    "v_cvt_pk_bf16_f32 v158, v92, v93\n" \
    "v_exp_f32 v95, v95\n" \
    "s_nop 0\n" \
    "v_cvt_pk_bf16_f32 v159, v94, v95\n" \
    "v_mfma_f32_32x32x16_bf16 v[80:95], v[168:171], v[164:167], v[128:143]\n" \
    "ds_read_b128 v[168:171], v180 offset:6144\n" \
    "v_exp_f32 v64, v64\n" \
    "v_exp_f32 v65, v65\n" \
    "v_exp_f32 v66, v66\n" \
    "v_cvt_pk_bf16_f32 v144, v64, v65\n" \
    "v_exp_f32 v67, v67\n" \
    "v_exp_f32 v68, v68\n" \
    "v_cvt_pk_bf16_f32 v145, v66, v67\n" \
    "v_mfma_f32_32x32x16_bf16 v[112:127], v[172:175], v[152:155], v[112:127]\n" \
    "v_exp_f32 v69, v69\n" \
    "v_exp_f32 v70, v70\n" \
    "v_cvt_pk_bf16_f32 v146, v68, v69\n" \
    "v_exp_f32 v71, v71\n" \
    "v_exp_f32 v72, v72\n" \
    "v_cvt_pk_bf16_f32 v147, v70, v71\n" \
    "v_exp_f32 v73, v73\n" \
    "v_exp_f32 v74, v74\n" \
    "v_cvt_pk_bf16_f32 v148, v72, v73\n" \
    "v_mfma_f32_32x32x16_bf16 v[112:127], v[176:179], v[156:159], v[112:127]\n" \
    "s_mov_b64 exec, s[48:49]\n" \
    "ds_read_b128 v[172:175], v181 offset:5120\n" \
    "ds_read_b128 v[176:179], v181 offset:5632\n" \
    "s_mov_b64 exec, -1\n" \
    "v_exp_f32 v75, v75\n" \
    "v_exp_f32 v76, v76\n" \
    "v_cvt_pk_bf16_f32 v149, v74, v75\n" \
    "v_exp_f32 v77, v77\n" \
    "v_exp_f32 v78, v78\n" \
    "v_cvt_pk_bf16_f32 v150, v76, v77\n" \
    "v_exp_f32 v79, v79\n" \
    "s_waitcnt lgkmcnt(2)\n" \
    "v_cvt_pk_bf16_f32 v151, v78, v79\n" \
    "v_mfma_f32_32x32x16_bf16 v[64:79], v[168:171], v[160:163], v[128:143]\n" \
    "v_exp_f32 v80, v80\n" \
    "v_exp_f32 v81, v81\n" \
    "v_exp_f32 v82, v82\n" \
    "v_cvt_pk_bf16_f32 v152, v80, v81\n" \
    "v_exp_f32 v83, v83\n" \
    "v_exp_f32 v84, v84\n" \
    "v_cvt_pk_bf16_f32 v153, v82, v83\n" \
    "s_waitcnt lgkmcnt(0)\n" \
    "v_mfma_f32_32x32x16_bf16 v[96:111], v[172:175], v[144:147], v[96:111]\n" \
    "v_exp_f32 v85, v85\n" \
    "v_exp_f32 v86, v86\n" \
    "v_cvt_pk_bf16_f32 v154, v84, v85\n" \
    "v_exp_f32 v87, v87\n" \
    "v_exp_f32 v88, v88\n" \
    "v_cvt_pk_bf16_f32 v155, v86, v87\n" \
    "v_exp_f32 v89, v89\n" \
    "v_exp_f32 v90, v90\n" \
    "v_cvt_pk_bf16_f32 v156, v88, v89\n" \
    "v_mfma_f32_32x32x16_bf16 v[96:111], v[176:179], v[148:151], v[96:111]\n" \
    "v_exp_f32 v91, v91\n" \
    "v_exp_f32 v92, v92\n" \
    "v_cvt_pk_bf16_f32 v157, v90, v91\n" \
    "v_exp_f32 v93, v93\n" \
    "v_exp_f32 v94, v94\n" \
    "v_cvt_pk_bf16_f32 v158, v92, v93\n" \
    "v_exp_f32 v95, v95\n" \
    "s_nop 0\n" \
    "v_cvt_pk_bf16_f32 v159, v94, v95\n" \
    "v_mfma_f32_32x32x16_bf16 v[80:95], v[168:171], v[164:167], v[128:143]\n" \
    "ds_read_b128 v[168:171], v180 offset:7168\n" \
    "v_exp_f32 v64, v64\n" \
    "v_exp_f32 v65, v65\n" \
    "v_exp_f32 v66, v66\n" \
    "v_cvt_pk_bf16_f32 v144, v64, v65\n" \
    "v_exp_f32 v67, v67\n" \
    "v_exp_f32 v68, v68\n" \
    "v_cvt_pk_bf16_f32 v145, v66, v67\n" \
    "v_mfma_f32_32x32x16_bf16 v[112:127], v[172:175], v[152:155], v[112:127]\n" \
    "v_exp_f32 v69, v69\n" \
    "v_exp_f32 v70, v70\n" \
    "v_cvt_pk_bf16_f32 v146, v68, v69\n" \
    "v_exp_f32 v71, v71\n" \
    "v_exp_f32 v72, v72\n" \
    "v_cvt_pk_bf16_f32 v147, v70, v71\n" \
    "v_exp_f32 v73, v73\n" \
    "v_exp_f32 v74, v74\n" \
    "v_cvt_pk_bf16_f32 v148, v72, v73\n" \
    "v_mfma_f32_32x32x16_bf16 v[112:127], v[176:179], v[156:159], v[112:127]\n" \
    "s_mov_b64 exec, s[48:49]\n" \
    "ds_read_b128 v[172:175], v181 offset:6144\n" \
    "ds_read_b128 v[176:179], v181 offset:6656\n" \
    "s_mov_b64 exec, -1\n" \
    "v_exp_f32 v75, v75\n" \
    "v_exp_f32 v76, v76\n" \
    "v_cvt_pk_bf16_f32 v149, v74, v75\n" \
    "v_exp_f32 v77, v77\n" \
    "v_exp_f32 v78, v78\n" \
    "v_cvt_pk_bf16_f32 v150, v76, v77\n" \
    "v_exp_f32 v79, v79\n" \
    "s_waitcnt lgkmcnt(2)\n" \
    "v_cvt_pk_bf16_f32 v151, v78, v79\n" \
    "v_mfma_f32_32x32x16_bf16 v[64:79], v[168:171], v[160:163], v[128:143]\n" \
    "v_exp_f32 v80, v80\n" \
    "v_exp_f32 v81, v81\n" \
    "v_exp_f32 v82, v82\n" \
    "v_cvt_pk_bf16_f32 v152, v80, v81\n" \
    "v_exp_f32 v83, v83\n" \
    "v_exp_f32 v84, v84\n" \
    "v_cvt_pk_bf16_f32 v153, v82, v83\n" \
    "s_waitcnt lgkmcnt(0)\n" \
    "v_mfma_f32_32x32x16_bf16 v[96:111], v[172:175], v[144:147], v[96:111]\n" \
    "v_exp_f32 v85, v85\n" \
    "v_exp_f32 v86, v86\n" \
    "v_cvt_pk_bf16_f32 v154, v84, v85\n" \
    "v_exp_f32 v87, v87\n" \
    "v_exp_f32 v88, v88\n" \
    "v_cvt_pk_bf16_f32 v155, v86, v87\n" \
    "v_exp_f32 v89, v89\n" \
    "v_exp_f32 v90, v90\n" \
    "v_cvt_pk_bf16_f32 v156, v88, v89\n" \
    "v_mfma_f32_32x32x16_bf16 v[96:111], v[176:179], v[148:151], v[96:111]\n" \
    "v_exp_f32 v91, v91\n" \
    "v_exp_f32 v92, v92\n" \
    "v_cvt_pk_bf16_f32 v157, v90, v91\n" \
    "v_exp_f32 v93, v93\n" \
    "v_exp_f32 v94, v94\n" \
    "v_cvt_pk_bf16_f32 v158, v92, v93\n" \
    "v_exp_f32 v95, v95\n" \
    "s_nop 0\n" \
    "v_cvt_pk_bf16_f32 v159, v94, v95\n" \
    "s_bitcmp1_b32 s46, s44\n" \
    "s_cbranch_scc1 L_hook_%=\n" \
    "L_back_%=:\n" \
    "v_mfma_f32_32x32x16_bf16 v[80:95], v[168:171], v[164:167], v[128:143]\n" \
    "ds_read_b128 v[168:171], v180 offset:8192\n" \
    "v_exp_f32 v64, v64\n" \
    "v_exp_f32 v65, v65\n" \
    "v_exp_f32 v66, v66\n" \
    "v_cvt_pk_bf16_f32 v144, v64, v65\n" \
    "v_exp_f32 v67, v67\n" \
    "v_exp_f32 v68, v68\n" \
    "v_cvt_pk_bf16_f32 v145, v66, v67\n" \
    "v_mfma_f32_32x32x16_bf16 v[112:127], v[172:175], v[152:155], v[112:127]\n" \
    "v_exp_f32 v69, v69\n" \
    "v_exp_f32 v70, v70\n" \
    "v_cvt_pk_bf16_f32 v146, v68, v69\n" \
    "v_exp_f32 v71, v71\n" \
    "v_exp_f32 v72, v72\n" \
    "v_cvt_pk_bf16_f32 v147, v70, v71\n" \
    "v_exp_f32 v73, v73\n" \
    "v_exp_f32 v74, v74\n" \
    "v_cvt_pk_bf16_f32 v148, v72, v73\n" \
    "v_mfma_f32_32x32x16_bf16 v[112:127], v[176:179], v[156:159], v[112:127]\n" \
    "s_mov_b64 exec, s[48:49]\n" \
    "ds_read_b128 v[172:175], v181 offset:7168\n" \
    "ds_read_b128 v[176:179], v181 offset:7680\n" \
    "s_mov_b64 exec, -1\n" \
    "v_exp_f32 v75, v75\n" \
    "v_exp_f32 v76, v76\n" \
    "v_cvt_pk_bf16_f32 v149, v74, v75\n" \
    "v_exp_f32 v77, v77\n" \
    "v_exp_f32 v78, v78\n" \
    "v_cvt_pk_bf16_f32 v150, v76, v77\n" \
    "v_exp_f32 v79, v79\n" \
    "s_waitcnt lgkmcnt(2)\n" \
    "v_cvt_pk_bf16_f32 v151, v78, v79\n" \
    "v_mfma_f32_32x32x16_bf16 v[64:79], v[168:171], v[160:163], v[128:143]\n" \
    "v_exp_f32 v80, v80\n" \
    "v_exp_f32 v81, v81\n" \
    "v_exp_f32 v82, v82\n" \
    "v_cvt_pk_bf16_f32 v152, v80, v81\n" \
    "v_exp_f32 v83, v83\n" \
    "v_exp_f32 v84, v84\n" \
    "v_cvt_pk_bf16_f32 v153, v82, v83\n" \
    "s_waitcnt lgkmcnt(0)\n" \
    "v_mfma_f32_32x32x16_bf16 v[96:111], v[172:175], v[144:147], v[96:111]\n" \
    "v_exp_f32 v85, v85\n" \
    "v_exp_f32 v86, v86\n" \
    "v_cvt_pk_bf16_f32 v154, v84, v85\n" \
    "v_exp_f32 v87, v87\n" \
    "v_exp_f32 v88, v88\n" \
    "v_cvt_pk_bf16_f32 v155, v86, v87\n" \
    "v_exp_f32 v89, v89\n" \
    "v_exp_f32 v90, v90\n" \
    "v_cvt_pk_bf16_f32 v156, v88, v89\n" \
    "v_mfma_f32_32x32x16_bf16 v[96:111], v[176:179], v[148:151], v[96:111]\n" \
    "v_exp_f32 v91, v91\n" \
    "v_exp_f32 v92, v92\n" \
    "v_cvt_pk_bf16_f32 v157, v90, v91\n" \
    "v_exp_f32 v93, v93\n" \
    "v_exp_f32 v94, v94\n" \
    "v_cvt_pk_bf16_f32 v158, v92, v93\n" \
    "v_exp_f32 v95, v95\n" \
    "s_nop 0\n" \
    "v_cvt_pk_bf16_f32 v159, v94, v95\n" \
    "v_add_u32 v180, 0x2000, v180\n" \
    "v_add_u32 v181, 0x2000, v181\n" \
    "s_add_u32 s44, s44, 1\n" \
    "s_cmp_lt_u32 s44, 8\n" \
    "s_cbranch_scc1 L_chunk_%=\n" \
    "v_mfma_f32_32x32x16_bf16 v[112:127], v[172:175], v[152:155], v[112:127]\n" \
    "v_mfma_f32_32x32x16_bf16 v[112:127], v[176:179], v[156:159], v[112:127]\n" \
    "s_branch L_done_%=\n" \
    "L_hook_%=:\n" \
    "s_bitcmp1_b32 s43, s44\n" \
    "s_cbranch_scc0 L_h1_%=\n" \
    "s_waitcnt lgkmcnt(0)\n" \
    "s_mov_b64 exec, 1\n" \
    "v_mov_b32 v183, 1\n" \
    "ds_add_u32 v185, v183\n" \
    "s_mov_b64 exec, -1\n" \
    "v_add_u32 v185, 4, v185\n" \
    "s_branch L_back_%=\n" \
    "L_h1_%=:\n" \
    "s_cmp_eq_u32 s44, 0\n" \
    "s_cbranch_scc0 L_h2_%=\n" \
    "s_waitcnt vmcnt(1)\n" \
    "s_mov_b64 exec, 1\n" \
    "v_mov_b32 v183, 1\n" \
    "ds_add_u32 v182, v183\n" \
    "s_mov_b64 exec, -1\n" \
    "L_h2_%=:\n" \
    "L_poll_%=:\n" \
    "ds_read_b32 v183, v182\n" \
    "s_waitcnt lgkmcnt(0)\n" \
    "v_cmp_gt_u32 vcc, 4, v183\n" \
    "s_cbranch_vccnz L_poll_%=\n" \
    "v_add_u32 v182, 4, v182\n" \
    "s_cmp_eq_u32 s44, 1\n" \
    "s_cbranch_scc0 L_back_%=\n" \
    "s_mov_b32 s45, m0\n" \
    "s_mov_b32 m0, s47\n" \
    "s_nop 0\n" \
    "global_load_lds_dword v[186:187], off\n" \
    "s_mov_b32 m0, s45\n" \
    "s_branch L_back_%=\n" \
    "L_done_%=:\n" \
    "s_nop 15\n" \
    "s_setprio 3\n"
#define ATTN_CLOB "v64", "v65", "v66", "v67", "v68", "v69", "v70", "v71", "v72", "v73", "v74", "v75", "v76", "v77", "v78", "v79", "v80", "v81", "v82", "v83", "v84", "v85", "v86", "v87", "v88", "v89", "v90", "v91", "v92", "v93", "v94", "v95", "v144", "v145", "v146", "v147", "v148", "v149", "v150", "v151", "v152", "v153", "v154", "v155", "v156", "v157", "v158", "v159", "v172", "v173", "v174", "v175", "v176", "v177", "v178", "v179", "v183", "v190", "v191", "v192", "v193", "v194", "v195", "v196", "v197", "v198", "v199", "s43", "s44", "s45", "s46", "s48", "s49", "vcc", "memory", "scc"

#define G1_PRE \
    "s_mov_b32 s63, m0\n" \
    "s_add_u32 m0, s62, 0\n" \
    "s_nop 0\n" \
    "global_load_lds_dwordx4 v176, s[56:57]\n" \
    "global_load_lds_dwordx4 v177, s[56:57] offset:1024\n" \
    "s_add_u32 s56, s56, 57344\n" \
    "s_addc_u32 s57, s57, 0\n" \
    "s_add_u32 m0, s62, 2048\n" \
    "s_nop 0\n" \
    "global_load_lds_dwordx4 v176, s[56:57]\n" \
    "global_load_lds_dwordx4 v177, s[56:57] offset:1024\n" \
    "s_add_u32 s56, s56, 57344\n" \
    "s_addc_u32 s57, s57, 0\n" \
    "s_add_u32 m0, s62, 4096\n" \
    "s_nop 0\n" \
    "global_load_lds_dwordx4 v176, s[56:57]\n" \
    "global_load_lds_dwordx4 v177, s[56:57] offset:1024\n" \
    "s_add_u32 s56, s56, 57344\n" \
    "s_addc_u32 s57, s57, 0\n" \
    "s_add_u32 m0, s62, 6144\n" \
    "s_nop 0\n" \
    "global_load_lds_dwordx4 v176, s[56:57]\n" \
    "global_load_lds_dwordx4 v177, s[56:57] offset:1024\n" \
    "s_add_u32 s56, s56, 57344\n" \
    "s_addc_u32 s57, s57, 0\n" \
    "s_mov_b32 m0, s63\n"
#define G1_ASM \
    "s_mov_b32 s63, m0\n" \
    "global_load_dwordx4 v[128:131], v178, s[58:59] offset:0\n" \
    "global_load_dwordx4 v[132:135], v178, s[60:61] offset:0\n" \
    "global_load_dwordx4 v[136:139], v178, s[58:59] offset:1024\n" \
    "global_load_dwordx4 v[140:143], v178, s[60:61] offset:1024\n" \
    "global_load_dwordx4 v[144:147], v178, s[58:59] offset:2048\n" \
    "global_load_dwordx4 v[148:151], v178, s[60:61] offset:2048\n" \
    "global_load_dwordx4 v[152:155], v178, s[58:59] offset:3072\n" \
    "global_load_dwordx4 v[156:159], v178, s[60:61] offset:3072\n" \
    "s_add_u32 s58, s58, 4096\n" \
    "s_addc_u32 s59, s59, 0\n" \
    "s_add_u32 s60, s60, 4096\n" \
    "s_addc_u32 s61, s61, 0\n" \
    "s_waitcnt vmcnt(6)\n" \
    "s_mov_b64 exec, 1\n" \
    "v_mov_b32 v160, 1\n" \
    "ds_add_u32 v181, v160\n" \
    "ds_add_u32 v181, v160 offset:4\n" \
    "s_mov_b64 exec, -1\n" \
    "s_waitcnt lgkmcnt(0)\n" \
    "ds_read_b64_tr_b16 v[160:161], v179 offset:0\n" \
    "ds_read_b64_tr_b16 v[162:163], v179 offset:512\n" \
    "ds_read_b64_tr_b16 v[164:165], v180 offset:0\n" \
    "ds_read_b64_tr_b16 v[166:167], v180 offset:512\n" \
    "s_waitcnt lgkmcnt(0)\n" \
    "v_mfma_f32_32x32x16_bf16 v[64:79], v[160:163], v[128:131], 0\n" \
    "s_add_u32 m0, s62, 0\n" \
    "s_nop 0\n" \
    "global_load_lds_dwordx4 v176, s[56:57]\n" \
    "global_load_lds_dwordx4 v177, s[56:57] offset:1024\n" \
    "s_add_u32 s56, s56, 57344\n" \
    "s_addc_u32 s57, s57, 0\n" \
    "v_mfma_f32_32x32x16_bf16 v[80:95], v[160:163], v[132:135], 0\n" \
    "s_waitcnt vmcnt(6)\n" \
    "ds_read_b64_tr_b16 v[168:169], v179 offset:2048\n" \
    "ds_read_b64_tr_b16 v[170:171], v179 offset:2560\n" \
    "ds_read_b64_tr_b16 v[172:173], v180 offset:2048\n" \
    "ds_read_b64_tr_b16 v[174:175], v180 offset:2560\n" \
    "v_mfma_f32_32x32x16_bf16 v[96:111], v[164:167], v[128:131], 0\n" \
    "v_mfma_f32_32x32x16_bf16 v[112:127], v[164:167], v[132:135], 0\n" \
    "global_load_dwordx4 v[128:131], v178, s[58:59] offset:0\n" \
    "global_load_dwordx4 v[132:135], v178, s[60:61] offset:0\n" \
    "s_waitcnt lgkmcnt(0)\n" \
    "v_mfma_f32_32x32x16_bf16 v[64:79], v[168:171], v[136:139], v[64:79]\n" \
    "s_add_u32 m0, s62, 2048\n" \
    "s_nop 0\n" \
    "global_load_lds_dwordx4 v176, s[56:57]\n" \
    "global_load_lds_dwordx4 v177, s[56:57] offset:1024\n" \
    "s_add_u32 s56, s56, 57344\n" \
    "s_addc_u32 s57, s57, 0\n" \
    "v_mfma_f32_32x32x16_bf16 v[80:95], v[168:171], v[140:143], v[80:95]\n" \
    "s_waitcnt vmcnt(8)\n" \
    "ds_read_b64_tr_b16 v[160:161], v179 offset:4096\n" \
    "ds_read_b64_tr_b16 v[162:163], v179 offset:4608\n" \
    "ds_read_b64_tr_b16 v[164:165], v180 offset:4096\n" \
    "ds_read_b64_tr_b16 v[166:167], v180 offset:4608\n" \
    "v_mfma_f32_32x32x16_bf16 v[96:111], v[172:175], v[136:139], v[96:111]\n" \
    "v_mfma_f32_32x32x16_bf16 v[112:127], v[172:175], v[140:143], v[112:127]\n" \
    "global_load_dwordx4 v[136:139], v178, s[58:59] offset:1024\n" \
    "global_load_dwordx4 v[140:143], v178, s[60:61] offset:1024\n" \
    "s_waitcnt lgkmcnt(0)\n" \
    "v_mfma_f32_32x32x16_bf16 v[64:79], v[160:163], v[144:147], v[64:79]\n" \
    "s_add_u32 m0, s62, 4096\n" \
    "s_nop 0\n" \
    "global_load_lds_dwordx4 v176, s[56:57]\n" \
    "global_load_lds_dwordx4 v177, s[56:57] offset:1024\n" \
    "s_add_u32 s56, s56, 57344\n" \
    "s_addc_u32 s57, s57, 0\n" \
    "v_mfma_f32_32x32x16_bf16 v[80:95], v[160:163], v[148:151], v[80:95]\n" \
    "s_waitcnt vmcnt(10)\n" \
    "ds_read_b64_tr_b16 v[168:169], v179 offset:6144\n" \
    "ds_read_b64_tr_b16 v[170:171], v179 offset:6656\n" \
    "ds_read_b64_tr_b16 v[172:173], v180 offset:6144\n" \
    "ds_read_b64_tr_b16 v[174:175], v180 offset:6656\n" \
    "v_mfma_f32_32x32x16_bf16 v[96:111], v[164:167], v[144:147], v[96:111]\n" \
    "v_mfma_f32_32x32x16_bf16 v[112:127], v[164:167], v[148:151], v[112:127]\n" \
    "global_load_dwordx4 v[144:147], v178, s[58:59] offset:2048\n" \
    "global_load_dwordx4 v[148:151], v178, s[60:61] offset:2048\n" \
    "s_waitcnt lgkmcnt(0)\n" \
    "v_mfma_f32_32x32x16_bf16 v[64:79], v[168:171], v[152:155], v[64:79]\n" \
    "s_add_u32 m0, s62, 6144\n" \
    "s_nop 0\n" \
    "global_load_lds_dwordx4 v176, s[56:57]\n" \
    "global_load_lds_dwordx4 v177, s[56:57] offset:1024\n" \
    "s_add_u32 s56, s56, 57344\n" \
    "s_addc_u32 s57, s57, 0\n" \
    "v_mfma_f32_32x32x16_bf16 v[80:95], v[168:171], v[156:159], v[80:95]\n" \
    "s_waitcnt vmcnt(10)\n" \
    "ds_read_b64_tr_b16 v[160:161], v179 offset:0\n" \
    "ds_read_b64_tr_b16 v[162:163], v179 offset:512\n" \
    "ds_read_b64_tr_b16 v[164:165], v180 offset:0\n" \
    "ds_read_b64_tr_b16 v[166:167], v180 offset:512\n" \
    "v_mfma_f32_32x32x16_bf16 v[96:111], v[172:175], v[152:155], v[96:111]\n" \
    "v_mfma_f32_32x32x16_bf16 v[112:127], v[172:175], v[156:159], v[112:127]\n" \
    "global_load_dwordx4 v[152:155], v178, s[58:59] offset:3072\n" \
    "global_load_dwordx4 v[156:159], v178, s[60:61] offset:3072\n" \
    "s_add_u32 s58, s58, 4096\n" \
    "s_addc_u32 s59, s59, 0\n" \
    "s_add_u32 s60, s60, 4096\n" \
    "s_addc_u32 s61, s61, 0\n" \
    "s_waitcnt lgkmcnt(0)\n" \
    "v_mfma_f32_32x32x16_bf16 v[64:79], v[160:163], v[128:131], v[64:79]\n" \
    "s_add_u32 m0, s62, 0\n" \
    "s_nop 0\n" \
    "global_load_lds_dwordx4 v176, s[56:57]\n" \
    "global_load_lds_dwordx4 v177, s[56:57] offset:1024\n" \
    "s_add_u32 s56, s56, 57344\n" \
    "s_addc_u32 s57, s57, 0\n" \
    "v_mfma_f32_32x32x16_bf16 v[80:95], v[160:163], v[132:135], v[80:95]\n" \
    "s_waitcnt vmcnt(10)\n" \
    "ds_read_b64_tr_b16 v[168:169], v179 offset:2048\n" \
    "ds_read_b64_tr_b16 v[170:171], v179 offset:2560\n" \
    "ds_read_b64_tr_b16 v[172:173], v180 offset:2048\n" \
    "ds_read_b64_tr_b16 v[174:175], v180 offset:2560\n" \
    "v_mfma_f32_32x32x16_bf16 v[96:111], v[164:167], v[128:131], v[96:111]\n" \
    "v_mfma_f32_32x32x16_bf16 v[112:127], v[164:167], v[132:135], v[112:127]\n" \
    "global_load_dwordx4 v[128:131], v178, s[58:59] offset:0\n" \
    "global_load_dwordx4 v[132:135], v178, s[60:61] offset:0\n" \
    "s_waitcnt lgkmcnt(0)\n" \
    "v_mfma_f32_32x32x16_bf16 v[64:79], v[168:171], v[136:139], v[64:79]\n" \
    "s_add_u32 m0, s62, 2048\n" \
    "s_nop 0\n" \
    "global_load_lds_dwordx4 v176, s[56:57]\n" \
    "global_load_lds_dwordx4 v177, s[56:57] offset:1024\n" \
    "s_add_u32 s56, s56, 57344\n" \
    "s_addc_u32 s57, s57, 0\n" \
    "v_mfma_f32_32x32x16_bf16 v[80:95], v[168:171], v[140:143], v[80:95]\n" \
    "s_waitcnt vmcnt(10)\n" \
    "ds_read_b64_tr_b16 v[160:161], v179 offset:4096\n" \
    "ds_read_b64_tr_b16 v[162:163], v179 offset:4608\n" \
    "ds_read_b64_tr_b16 v[164:165], v180 offset:4096\n" \
    "ds_read_b64_tr_b16 v[166:167], v180 offset:4608\n" \
    "v_mfma_f32_32x32x16_bf16 v[96:111], v[172:175], v[136:139], v[96:111]\n" \
    "v_mfma_f32_32x32x16_bf16 v[112:127], v[172:175], v[140:143], v[112:127]\n" \
    "global_load_dwordx4 v[136:139], v178, s[58:59] offset:1024\n" \
    "global_load_dwordx4 v[140:143], v178, s[60:61] offset:1024\n" \
    "s_waitcnt lgkmcnt(0)\n" \
    "v_mfma_f32_32x32x16_bf16 v[64:79], v[160:163], v[144:147], v[64:79]\n" \
    "s_add_u32 m0, s62, 4096\n" \
    "s_nop 0\n" \
    "global_load_lds_dwordx4 v176, s[56:57]\n" \
    "global_load_lds_dwordx4 v177, s[56:57] offset:1024\n" \
    "s_add_u32 s56, s56, 57344\n" \
    "s_addc_u32 s57, s57, 0\n" \
    "v_mfma_f32_32x32x16_bf16 v[80:95], v[160:163], v[148:151], v[80:95]\n" \
    "s_waitcnt vmcnt(10)\n" \
    "ds_read_b64_tr_b16 v[168:169], v179 offset:6144\n" \
    "ds_read_b64_tr_b16 v[170:171], v179 offset:6656\n" \
    "ds_read_b64_tr_b16 v[172:173], v180 offset:6144\n" \
    "ds_read_b64_tr_b16 v[174:175], v180 offset:6656\n" \
    "v_mfma_f32_32x32x16_bf16 v[96:111], v[164:167], v[144:147], v[96:111]\n" \
    "v_mfma_f32_32x32x16_bf16 v[112:127], v[164:167], v[148:151], v[112:127]\n" \
    "global_load_dwordx4 v[144:147], v178, s[58:59] offset:2048\n" \
    "global_load_dwordx4 v[148:151], v178, s[60:61] offset:2048\n" \
    "s_waitcnt lgkmcnt(0)\n" \
    "v_mfma_f32_32x32x16_bf16 v[64:79], v[168:171], v[152:155], v[64:79]\n" \
    "s_add_u32 m0, s62, 6144\n" \
    "s_nop 0\n" \
    "global_load_lds_dwordx4 v176, s[56:57]\n" \
    "global_load_lds_dwordx4 v177, s[56:57] offset:1024\n" \
    "s_add_u32 s56, s56, 57344\n" \
    "s_addc_u32 s57, s57, 0\n" \
    "v_mfma_f32_32x32x16_bf16 v[80:95], v[168:171], v[156:159], v[80:95]\n" \
    "s_waitcnt vmcnt(10)\n" \
    "ds_read_b64_tr_b16 v[160:161], v179 offset:0\n" \
    "ds_read_b64_tr_b16 v[162:163], v179 offset:512\n" \
    "ds_read_b64_tr_b16 v[164:165], v180 offset:0\n" \
    "ds_read_b64_tr_b16 v[166:167], v180 offset:512\n" \
    "v_mfma_f32_32x32x16_bf16 v[96:111], v[172:175], v[152:155], v[96:111]\n" \
    "v_mfma_f32_32x32x16_bf16 v[112:127], v[172:175], v[156:159], v[112:127]\n" \
    "global_load_dwordx4 v[152:155], v178, s[58:59] offset:3072\n" \
    "global_load_dwordx4 v[156:159], v178, s[60:61] offset:3072\n" \
    "s_add_u32 s58, s58, 4096\n" \
    "s_addc_u32 s59, s59, 0\n" \
    "s_add_u32 s60, s60, 4096\n" \
    "s_addc_u32 s61, s61, 0\n" \
    "s_waitcnt lgkmcnt(0)\n" \
    "v_mfma_f32_32x32x16_bf16 v[64:79], v[160:163], v[128:131], v[64:79]\n" \
    "s_add_u32 m0, s62, 0\n" \
    "s_nop 0\n" \
    "global_load_lds_dwordx4 v176, s[56:57]\n" \
    "global_load_lds_dwordx4 v177, s[56:57] offset:1024\n" \
    "s_add_u32 s56, s56, 57344\n" \
    "s_addc_u32 s57, s57, 0\n" \
    "v_mfma_f32_32x32x16_bf16 v[80:95], v[160:163], v[132:135], v[80:95]\n" \
    "s_waitcnt vmcnt(10)\n" \
    "ds_read_b64_tr_b16 v[168:169], v179 offset:2048\n" \
    "ds_read_b64_tr_b16 v[170:171], v179 offset:2560\n" \
    "ds_read_b64_tr_b16 v[172:173], v180 offset:2048\n" \
    "ds_read_b64_tr_b16 v[174:175], v180 offset:2560\n" \
    "v_mfma_f32_32x32x16_bf16 v[96:111], v[164:167], v[128:131], v[96:111]\n" \
    "v_mfma_f32_32x32x16_bf16 v[112:127], v[164:167], v[132:135], v[112:127]\n" \
    "global_load_dwordx4 v[128:131], v178, s[58:59] offset:0\n" \
    "global_load_dwordx4 v[132:135], v178, s[60:61] offset:0\n" \
    "s_waitcnt lgkmcnt(0)\n" \
    "v_mfma_f32_32x32x16_bf16 v[64:79], v[168:171], v[136:139], v[64:79]\n" \
    "s_add_u32 m0, s62, 2048\n" \
    "s_nop 0\n" \
    "global_load_lds_dwordx4 v176, s[56:57]\n" \
    "global_load_lds_dwordx4 v177, s[56:57] offset:1024\n" \
    "s_add_u32 s56, s56, 57344\n" \
    "s_addc_u32 s57, s57, 0\n" \
    "v_mfma_f32_32x32x16_bf16 v[80:95], v[168:171], v[140:143], v[80:95]\n" \
    "s_waitcnt vmcnt(10)\n" \
    "ds_read_b64_tr_b16 v[160:161], v179 offset:4096\n" \
    "ds_read_b64_tr_b16 v[162:163], v179 offset:4608\n" \
    "ds_read_b64_tr_b16 v[164:165], v180 offset:4096\n" \
    "ds_read_b64_tr_b16 v[166:167], v180 offset:4608\n" \
    "v_mfma_f32_32x32x16_bf16 v[96:111], v[172:175], v[136:139], v[96:111]\n" \
    "v_mfma_f32_32x32x16_bf16 v[112:127], v[172:175], v[140:143], v[112:127]\n" \
    "global_load_dwordx4 v[136:139], v178, s[58:59] offset:1024\n" \
    "global_load_dwordx4 v[140:143], v178, s[60:61] offset:1024\n" \
    "s_waitcnt lgkmcnt(0)\n" \
    "v_mfma_f32_32x32x16_bf16 v[64:79], v[160:163], v[144:147], v[64:79]\n" \
    "s_add_u32 m0, s62, 4096\n" \
    "s_nop 0\n" \
    "global_load_lds_dwordx4 v176, s[56:57]\n" \
    "global_load_lds_dwordx4 v177, s[56:57] offset:1024\n" \
    "s_add_u32 s56, s56, 57344\n" \
    "s_addc_u32 s57, s57, 0\n" \
    "v_mfma_f32_32x32x16_bf16 v[80:95], v[160:163], v[148:151], v[80:95]\n" \
    "s_waitcnt vmcnt(10)\n" \
    "ds_read_b64_tr_b16 v[168:169], v179 offset:6144\n" \
    "ds_read_b64_tr_b16 v[170:171], v179 offset:6656\n" \
    "ds_read_b64_tr_b16 v[172:173], v180 offset:6144\n" \
    "ds_read_b64_tr_b16 v[174:175], v180 offset:6656\n" \
    "v_mfma_f32_32x32x16_bf16 v[96:111], v[164:167], v[144:147], v[96:111]\n" \
    "v_mfma_f32_32x32x16_bf16 v[112:127], v[164:167], v[148:151], v[112:127]\n" \
    "global_load_dwordx4 v[144:147], v178, s[58:59] offset:2048\n" \
    "global_load_dwordx4 v[148:151], v178, s[60:61] offset:2048\n" \
    "s_waitcnt lgkmcnt(0)\n" \
    "v_mfma_f32_32x32x16_bf16 v[64:79], v[168:171], v[152:155], v[64:79]\n" \
    "s_add_u32 m0, s62, 6144\n" \
    "s_nop 0\n" \
    "global_load_lds_dwordx4 v176, s[56:57]\n" \
    "global_load_lds_dwordx4 v177, s[56:57] offset:1024\n" \
    "s_add_u32 s56, s56, 57344\n" \
    "s_addc_u32 s57, s57, 0\n" \
    "v_mfma_f32_32x32x16_bf16 v[80:95], v[168:171], v[156:159], v[80:95]\n" \
    "s_waitcnt vmcnt(10)\n" \
    "ds_read_b64_tr_b16 v[160:161], v179 offset:0\n" \
    "ds_read_b64_tr_b16 v[162:163], v179 offset:512\n" \
    "ds_read_b64_tr_b16 v[164:165], v180 offset:0\n" \
    "ds_read_b64_tr_b16 v[166:167], v180 offset:512\n" \
    "v_mfma_f32_32x32x16_bf16 v[96:111], v[172:175], v[152:155], v[96:111]\n" \
    "v_mfma_f32_32x32x16_bf16 v[112:127], v[172:175], v[156:159], v[112:127]\n" \
    "global_load_dwordx4 v[152:155], v178, s[58:59] offset:3072\n" \
    "global_load_dwordx4 v[156:159], v178, s[60:61] offset:3072\n" \
    "s_add_u32 s58, s58, 4096\n" \
    "s_addc_u32 s59, s59, 0\n" \
    "s_add_u32 s60, s60, 4096\n" \
    "s_addc_u32 s61, s61, 0\n" \
    "s_waitcnt lgkmcnt(0)\n" \
    "v_mfma_f32_32x32x16_bf16 v[64:79], v[160:163], v[128:131], v[64:79]\n" \
    "s_add_u32 m0, s62, 0\n" \
    "s_nop 0\n" \
    "global_load_lds_dwordx4 v176, s[56:57]\n" \
    "global_load_lds_dwordx4 v177, s[56:57] offset:1024\n" \
    "s_add_u32 s56, s56, 57344\n" \
    "s_addc_u32 s57, s57, 0\n" \
    "v_mfma_f32_32x32x16_bf16 v[80:95], v[160:163], v[132:135], v[80:95]\n" \
    "s_waitcnt vmcnt(10)\n" \
    "ds_read_b64_tr_b16 v[168:169], v179 offset:2048\n" \
    "ds_read_b64_tr_b16 v[170:171], v179 offset:2560\n" \
    "ds_read_b64_tr_b16 v[172:173], v180 offset:2048\n" \
    "ds_read_b64_tr_b16 v[174:175], v180 offset:2560\n" \
    "v_mfma_f32_32x32x16_bf16 v[96:111], v[164:167], v[128:131], v[96:111]\n" \
    "v_mfma_f32_32x32x16_bf16 v[112:127], v[164:167], v[132:135], v[112:127]\n" \
    "global_load_dwordx4 v[128:131], v178, s[58:59] offset:0\n" \
    "global_load_dwordx4 v[132:135], v178, s[60:61] offset:0\n" \
    "s_waitcnt lgkmcnt(0)\n" \
    "v_mfma_f32_32x32x16_bf16 v[64:79], v[168:171], v[136:139], v[64:79]\n" \
    "s_add_u32 m0, s62, 2048\n" \
    "s_nop 0\n" \
    "global_load_lds_dwordx4 v176, s[56:57]\n" \
    "global_load_lds_dwordx4 v177, s[56:57] offset:1024\n" \
    "s_add_u32 s56, s56, 57344\n" \
    "s_addc_u32 s57, s57, 0\n" \
    "v_mfma_f32_32x32x16_bf16 v[80:95], v[168:171], v[140:143], v[80:95]\n" \
    "s_waitcnt vmcnt(10)\n" \
    "ds_read_b64_tr_b16 v[160:161], v179 offset:4096\n" \
    "ds_read_b64_tr_b16 v[162:163], v179 offset:4608\n" \
    "ds_read_b64_tr_b16 v[164:165], v180 offset:4096\n" \
    "ds_read_b64_tr_b16 v[166:167], v180 offset:4608\n" \
    "v_mfma_f32_32x32x16_bf16 v[96:111], v[172:175], v[136:139], v[96:111]\n" \
    "v_mfma_f32_32x32x16_bf16 v[112:127], v[172:175], v[140:143], v[112:127]\n" \
    "global_load_dwordx4 v[136:139], v178, s[58:59] offset:1024\n" \
    "global_load_dwordx4 v[140:143], v178, s[60:61] offset:1024\n" \
    "s_waitcnt lgkmcnt(0)\n" \
    "v_mfma_f32_32x32x16_bf16 v[64:79], v[160:163], v[144:147], v[64:79]\n" \
    "s_add_u32 m0, s62, 4096\n" \
    "s_nop 0\n" \
    "global_load_lds_dwordx4 v176, s[56:57]\n" \
    "global_load_lds_dwordx4 v177, s[56:57] offset:1024\n" \
    "s_add_u32 s56, s56, 57344\n" \
    "s_addc_u32 s57, s57, 0\n" \
    "v_mfma_f32_32x32x16_bf16 v[80:95], v[160:163], v[148:151], v[80:95]\n" \
    "s_waitcnt vmcnt(10)\n" \
    "ds_read_b64_tr_b16 v[168:169], v179 offset:6144\n" \
    "ds_read_b64_tr_b16 v[170:171], v179 offset:6656\n" \
    "ds_read_b64_tr_b16 v[172:173], v180 offset:6144\n" \
    "ds_read_b64_tr_b16 v[174:175], v180 offset:6656\n" \
    "v_mfma_f32_32x32x16_bf16 v[96:111], v[164:167], v[144:147], v[96:111]\n" \
    "v_mfma_f32_32x32x16_bf16 v[112:127], v[164:167], v[148:151], v[112:127]\n" \
    "global_load_dwordx4 v[144:147], v178, s[58:59] offset:2048\n" \
    "global_load_dwordx4 v[148:151], v178, s[60:61] offset:2048\n" \
    "s_waitcnt lgkmcnt(0)\n" \
    "v_mfma_f32_32x32x16_bf16 v[64:79], v[168:171], v[152:155], v[64:79]\n" \
    "s_add_u32 m0, s62, 6144\n" \
    "s_nop 0\n" \
    "global_load_lds_dwordx4 v176, s[56:57]\n" \
    "global_load_lds_dwordx4 v177, s[56:57] offset:1024\n" \
    "s_add_u32 s56, s56, 57344\n" \
    "s_addc_u32 s57, s57, 0\n" \
    "v_mfma_f32_32x32x16_bf16 v[80:95], v[168:171], v[156:159], v[80:95]\n" \
    "s_waitcnt vmcnt(10)\n" \
    "ds_read_b64_tr_b16 v[160:161], v179 offset:0\n" \
    "ds_read_b64_tr_b16 v[162:163], v179 offset:512\n" \
    "ds_read_b64_tr_b16 v[164:165], v180 offset:0\n" \
    "ds_read_b64_tr_b16 v[166:167], v180 offset:512\n" \
    "v_mfma_f32_32x32x16_bf16 v[96:111], v[172:175], v[152:155], v[96:111]\n" \
    "v_mfma_f32_32x32x16_bf16 v[112:127], v[172:175], v[156:159], v[112:127]\n" \
    "global_load_dwordx4 v[152:155], v178, s[58:59] offset:3072\n" \
    "global_load_dwordx4 v[156:159], v178, s[60:61] offset:3072\n" \
    "s_add_u32 s58, s58, 4096\n" \
    "s_addc_u32 s59, s59, 0\n" \
    "s_add_u32 s60, s60, 4096\n" \
    "s_addc_u32 s61, s61, 0\n" \
    "s_waitcnt lgkmcnt(0)\n" \
    "v_mfma_f32_32x32x16_bf16 v[64:79], v[160:163], v[128:131], v[64:79]\n" \
    "s_add_u32 m0, s62, 0\n" \
    "s_nop 0\n" \
    "global_load_lds_dwordx4 v176, s[56:57]\n" \
    "global_load_lds_dwordx4 v177, s[56:57] offset:1024\n" \
    "s_add_u32 s56, s56, 57344\n" \
    "s_addc_u32 s57, s57, 0\n" \
    "v_mfma_f32_32x32x16_bf16 v[80:95], v[160:163], v[132:135], v[80:95]\n" \
    "s_waitcnt vmcnt(10)\n" \
    "ds_read_b64_tr_b16 v[168:169], v179 offset:2048\n" \
    "ds_read_b64_tr_b16 v[170:171], v179 offset:2560\n" \
    "ds_read_b64_tr_b16 v[172:173], v180 offset:2048\n" \
    "ds_read_b64_tr_b16 v[174:175], v180 offset:2560\n" \
    "v_mfma_f32_32x32x16_bf16 v[96:111], v[164:167], v[128:131], v[96:111]\n" \
    "v_mfma_f32_32x32x16_bf16 v[112:127], v[164:167], v[132:135], v[112:127]\n" \
    "global_load_dwordx4 v[128:131], v178, s[58:59] offset:0\n" \
    "global_load_dwordx4 v[132:135], v178, s[60:61] offset:0\n" \
    "s_waitcnt lgkmcnt(0)\n" \
    "v_mfma_f32_32x32x16_bf16 v[64:79], v[168:171], v[136:139], v[64:79]\n" \
    "s_add_u32 m0, s62, 2048\n" \
    "s_nop 0\n" \
    "global_load_lds_dwordx4 v176, s[56:57]\n" \
    "global_load_lds_dwordx4 v177, s[56:57] offset:1024\n" \
    "s_add_u32 s56, s56, 57344\n" \
    "s_addc_u32 s57, s57, 0\n" \
    "v_mfma_f32_32x32x16_bf16 v[80:95], v[168:171], v[140:143], v[80:95]\n" \
    "s_waitcnt vmcnt(10)\n" \
    "ds_read_b64_tr_b16 v[160:161], v179 offset:4096\n" \
    "ds_read_b64_tr_b16 v[162:163], v179 offset:4608\n" \
    "ds_read_b64_tr_b16 v[164:165], v180 offset:4096\n" \
    "ds_read_b64_tr_b16 v[166:167], v180 offset:4608\n" \
    "v_mfma_f32_32x32x16_bf16 v[96:111], v[172:175], v[136:139], v[96:111]\n" \
    "v_mfma_f32_32x32x16_bf16 v[112:127], v[172:175], v[140:143], v[112:127]\n" \
    "global_load_dwordx4 v[136:139], v178, s[58:59] offset:1024\n" \
    "global_load_dwordx4 v[140:143], v178, s[60:61] offset:1024\n" \
    "s_waitcnt lgkmcnt(0)\n" \
    "v_mfma_f32_32x32x16_bf16 v[64:79], v[160:163], v[144:147], v[64:79]\n" \
    "s_add_u32 m0, s62, 4096\n" \
    "s_nop 0\n" \
    "global_load_lds_dwordx4 v176, s[56:57]\n" \
    "global_load_lds_dwordx4 v177, s[56:57] offset:1024\n" \
    "s_add_u32 s56, s56, 57344\n" \
    "s_addc_u32 s57, s57, 0\n" \
    "v_mfma_f32_32x32x16_bf16 v[80:95], v[160:163], v[148:151], v[80:95]\n" \
    "s_waitcnt vmcnt(10)\n" \
    "ds_read_b64_tr_b16 v[168:169], v179 offset:6144\n" \
    "ds_read_b64_tr_b16 v[170:171], v179 offset:6656\n" \
    "ds_read_b64_tr_b16 v[172:173], v180 offset:6144\n" \
    "ds_read_b64_tr_b16 v[174:175], v180 offset:6656\n" \
    "v_mfma_f32_32x32x16_bf16 v[96:111], v[164:167], v[144:147], v[96:111]\n" \
    "v_mfma_f32_32x32x16_bf16 v[112:127], v[164:167], v[148:151], v[112:127]\n" \
    "global_load_dwordx4 v[144:147], v178, s[58:59] offset:2048\n" \
    "global_load_dwordx4 v[148:151], v178, s[60:61] offset:2048\n" \
    "s_waitcnt lgkmcnt(0)\n" \
    "v_mfma_f32_32x32x16_bf16 v[64:79], v[168:171], v[152:155], v[64:79]\n" \
    "s_add_u32 m0, s62, 6144\n" \
    "s_nop 0\n" \
    "global_load_lds_dwordx4 v176, s[56:57]\n" \
    "global_load_lds_dwordx4 v177, s[56:57] offset:1024\n" \
    "s_add_u32 s56, s56, 57344\n" \
    "s_addc_u32 s57, s57, 0\n" \
    "v_mfma_f32_32x32x16_bf16 v[80:95], v[168:171], v[156:159], v[80:95]\n" \
    "s_waitcnt vmcnt(10)\n" \
    "ds_read_b64_tr_b16 v[160:161], v179 offset:0\n" \
    "ds_read_b64_tr_b16 v[162:163], v179 offset:512\n" \
    "ds_read_b64_tr_b16 v[164:165], v180 offset:0\n" \
    "ds_read_b64_tr_b16 v[166:167], v180 offset:512\n" \
    "v_mfma_f32_32x32x16_bf16 v[96:111], v[172:175], v[152:155], v[96:111]\n" \
    "v_mfma_f32_32x32x16_bf16 v[112:127], v[172:175], v[156:159], v[112:127]\n" \
    "global_load_dwordx4 v[152:155], v178, s[58:59] offset:3072\n" \
    "global_load_dwordx4 v[156:159], v178, s[60:61] offset:3072\n" \
    "s_add_u32 s58, s58, 4096\n" \
    "s_addc_u32 s59, s59, 0\n" \
    "s_add_u32 s60, s60, 4096\n" \
    "s_addc_u32 s61, s61, 0\n" \
    "s_waitcnt lgkmcnt(0)\n" \
    "v_mfma_f32_32x32x16_bf16 v[64:79], v[160:163], v[128:131], v[64:79]\n" \
    "s_add_u32 m0, s62, 0\n" \
    "s_nop 0\n" \
    "global_load_lds_dwordx4 v176, s[56:57]\n" \
    "global_load_lds_dwordx4 v177, s[56:57] offset:1024\n" \
    "s_add_u32 s56, s56, 57344\n" \
    "s_addc_u32 s57, s57, 0\n" \
    "v_mfma_f32_32x32x16_bf16 v[80:95], v[160:163], v[132:135], v[80:95]\n" \
    "s_waitcnt vmcnt(10)\n" \
    "ds_read_b64_tr_b16 v[168:169], v179 offset:2048\n" \
    "ds_read_b64_tr_b16 v[170:171], v179 offset:2560\n" \
    "ds_read_b64_tr_b16 v[172:173], v180 offset:2048\n" \
    "ds_read_b64_tr_b16 v[174:175], v180 offset:2560\n" \
    "v_mfma_f32_32x32x16_bf16 v[96:111], v[164:167], v[128:131], v[96:111]\n" \
    "v_mfma_f32_32x32x16_bf16 v[112:127], v[164:167], v[132:135], v[112:127]\n" \
    "global_load_dwordx4 v[128:131], v178, s[58:59] offset:0\n" \
    "global_load_dwordx4 v[132:135], v178, s[60:61] offset:0\n" \
    "s_waitcnt lgkmcnt(0)\n" \
    "v_mfma_f32_32x32x16_bf16 v[64:79], v[168:171], v[136:139], v[64:79]\n" \
    "s_add_u32 m0, s62, 2048\n" \
    "s_nop 0\n" \
    "global_load_lds_dwordx4 v176, s[56:57]\n" \
    "global_load_lds_dwordx4 v177, s[56:57] offset:1024\n" \
    "s_add_u32 s56, s56, 57344\n" \
    "s_addc_u32 s57, s57, 0\n" \
    "v_mfma_f32_32x32x16_bf16 v[80:95], v[168:171], v[140:143], v[80:95]\n" \
    "s_waitcnt vmcnt(10)\n" \
    "ds_read_b64_tr_b16 v[160:161], v179 offset:4096\n" \
    "ds_read_b64_tr_b16 v[162:163], v179 offset:4608\n" \
    "ds_read_b64_tr_b16 v[164:165], v180 offset:4096\n" \
    "ds_read_b64_tr_b16 v[166:167], v180 offset:4608\n" \
    "v_mfma_f32_32x32x16_bf16 v[96:111], v[172:175], v[136:139], v[96:111]\n" \
    "v_mfma_f32_32x32x16_bf16 v[112:127], v[172:175], v[140:143], v[112:127]\n" \
    "global_load_dwordx4 v[136:139], v178, s[58:59] offset:1024\n" \
    "global_load_dwordx4 v[140:143], v178, s[60:61] offset:1024\n" \
    "s_waitcnt lgkmcnt(0)\n" \
    "v_mfma_f32_32x32x16_bf16 v[64:79], v[160:163], v[144:147], v[64:79]\n" \
    "s_add_u32 m0, s62, 4096\n" \
    "s_nop 0\n" \
    "global_load_lds_dwordx4 v176, s[56:57]\n" \
    "global_load_lds_dwordx4 v177, s[56:57] offset:1024\n" \
    "s_add_u32 s56, s56, 57344\n" \
    "s_addc_u32 s57, s57, 0\n" \
    "v_mfma_f32_32x32x16_bf16 v[80:95], v[160:163], v[148:151], v[80:95]\n" \
    "s_waitcnt vmcnt(10)\n" \
    "ds_read_b64_tr_b16 v[168:169], v179 offset:6144\n" \
    "ds_read_b64_tr_b16 v[170:171], v179 offset:6656\n" \
    "ds_read_b64_tr_b16 v[172:173], v180 offset:6144\n" \
    "ds_read_b64_tr_b16 v[174:175], v180 offset:6656\n" \
    "v_mfma_f32_32x32x16_bf16 v[96:111], v[164:167], v[144:147], v[96:111]\n" \
    "v_mfma_f32_32x32x16_bf16 v[112:127], v[164:167], v[148:151], v[112:127]\n" \
    "global_load_dwordx4 v[144:147], v178, s[58:59] offset:2048\n" \
    "global_load_dwordx4 v[148:151], v178, s[60:61] offset:2048\n" \
    "s_waitcnt lgkmcnt(0)\n" \
    "v_mfma_f32_32x32x16_bf16 v[64:79], v[168:171], v[152:155], v[64:79]\n" \
    "s_add_u32 m0, s62, 6144\n" \
    "s_nop 0\n" \
    "global_load_lds_dwordx4 v176, s[56:57]\n" \
    "global_load_lds_dwordx4 v177, s[56:57] offset:1024\n" \
    "s_add_u32 s56, s56, 57344\n" \
    "s_addc_u32 s57, s57, 0\n" \
    "v_mfma_f32_32x32x16_bf16 v[80:95], v[168:171], v[156:159], v[80:95]\n" \
    "s_waitcnt vmcnt(10)\n" \
    "ds_read_b64_tr_b16 v[160:161], v179 offset:0\n" \
    "ds_read_b64_tr_b16 v[162:163], v179 offset:512\n" \
    "ds_read_b64_tr_b16 v[164:165], v180 offset:0\n" \
    "ds_read_b64_tr_b16 v[166:167], v180 offset:512\n" \
    "v_mfma_f32_32x32x16_bf16 v[96:111], v[172:175], v[152:155], v[96:111]\n" \
    "v_mfma_f32_32x32x16_bf16 v[112:127], v[172:175], v[156:159], v[112:127]\n" \
    "global_load_dwordx4 v[152:155], v178, s[58:59] offset:3072\n" \
    "global_load_dwordx4 v[156:159], v178, s[60:61] offset:3072\n" \
    "s_add_u32 s58, s58, 4096\n" \
    "s_addc_u32 s59, s59, 0\n" \
    "s_add_u32 s60, s60, 4096\n" \
    "s_addc_u32 s61, s61, 0\n" \
    "s_waitcnt lgkmcnt(0)\n" \
    "v_mfma_f32_32x32x16_bf16 v[64:79], v[160:163], v[128:131], v[64:79]\n" \
    "s_add_u32 m0, s62, 0\n" \
    "s_nop 0\n" \
    "global_load_lds_dwordx4 v176, s[56:57]\n" \
    "global_load_lds_dwordx4 v177, s[56:57] offset:1024\n" \
    "s_add_u32 s56, s56, 57344\n" \
    "s_addc_u32 s57, s57, 0\n" \
    "v_mfma_f32_32x32x16_bf16 v[80:95], v[160:163], v[132:135], v[80:95]\n" \
    "s_waitcnt vmcnt(10)\n" \
    "ds_read_b64_tr_b16 v[168:169], v179 offset:2048\n" \
    "ds_read_b64_tr_b16 v[170:171], v179 offset:2560\n" \
    "ds_read_b64_tr_b16 v[172:173], v180 offset:2048\n" \
    "ds_read_b64_tr_b16 v[174:175], v180 offset:2560\n" \
    "v_mfma_f32_32x32x16_bf16 v[96:111], v[164:167], v[128:131], v[96:111]\n" \
    "v_mfma_f32_32x32x16_bf16 v[112:127], v[164:167], v[132:135], v[112:127]\n" \
    "global_load_dwordx4 v[128:131], v178, s[58:59] offset:0\n" \
    "global_load_dwordx4 v[132:135], v178, s[60:61] offset:0\n" \
    "s_waitcnt lgkmcnt(0)\n" \
    "v_mfma_f32_32x32x16_bf16 v[64:79], v[168:171], v[136:139], v[64:79]\n" \
    "s_add_u32 m0, s62, 2048\n" \
    "s_nop 0\n" \
    "global_load_lds_dwordx4 v176, s[56:57]\n" \
    "global_load_lds_dwordx4 v177, s[56:57] offset:1024\n" \
    "s_add_u32 s56, s56, 57344\n" \
    "s_addc_u32 s57, s57, 0\n" \
    "v_mfma_f32_32x32x16_bf16 v[80:95], v[168:171], v[140:143], v[80:95]\n" \
    "s_waitcnt vmcnt(10)\n" \
    "ds_read_b64_tr_b16 v[160:161], v179 offset:4096\n" \
    "ds_read_b64_tr_b16 v[162:163], v179 offset:4608\n" \
    "ds_read_b64_tr_b16 v[164:165], v180 offset:4096\n" \
    "ds_read_b64_tr_b16 v[166:167], v180 offset:4608\n" \
    "v_mfma_f32_32x32x16_bf16 v[96:111], v[172:175], v[136:139], v[96:111]\n" \
    "v_mfma_f32_32x32x16_bf16 v[112:127], v[172:175], v[140:143], v[112:127]\n" \
    "global_load_dwordx4 v[136:139], v178, s[58:59] offset:1024\n" \
    "global_load_dwordx4 v[140:143], v178, s[60:61] offset:1024\n" \
    "s_waitcnt lgkmcnt(0)\n" \
    "v_mfma_f32_32x32x16_bf16 v[64:79], v[160:163], v[144:147], v[64:79]\n" \
    "s_add_u32 m0, s62, 4096\n" \
    "s_nop 0\n" \
    "global_load_lds_dwordx4 v176, s[56:57]\n" \
    "global_load_lds_dwordx4 v177, s[56:57] offset:1024\n" \
    "s_add_u32 s56, s56, 57344\n" \
    "s_addc_u32 s57, s57, 0\n" \
    "v_mfma_f32_32x32x16_bf16 v[80:95], v[160:163], v[148:151], v[80:95]\n" \
    "s_waitcnt vmcnt(10)\n" \
    "ds_read_b64_tr_b16 v[168:169], v179 offset:6144\n" \
    "ds_read_b64_tr_b16 v[170:171], v179 offset:6656\n" \
    "ds_read_b64_tr_b16 v[172:173], v180 offset:6144\n" \
    "ds_read_b64_tr_b16 v[174:175], v180 offset:6656\n" \
    "v_mfma_f32_32x32x16_bf16 v[96:111], v[164:167], v[144:147], v[96:111]\n" \
    "v_mfma_f32_32x32x16_bf16 v[112:127], v[164:167], v[148:151], v[112:127]\n" \
    "global_load_dwordx4 v[144:147], v178, s[58:59] offset:2048\n" \
    "global_load_dwordx4 v[148:151], v178, s[60:61] offset:2048\n" \
    "s_waitcnt lgkmcnt(0)\n" \
    "v_mfma_f32_32x32x16_bf16 v[64:79], v[168:171], v[152:155], v[64:79]\n" \
    "s_add_u32 m0, s62, 6144\n" \
    "s_nop 0\n" \
    "global_load_lds_dwordx4 v176, s[56:57]\n" \
    "global_load_lds_dwordx4 v177, s[56:57] offset:1024\n" \
    "s_add_u32 s56, s56, 57344\n" \
    "s_addc_u32 s57, s57, 0\n" \
    "v_mfma_f32_32x32x16_bf16 v[80:95], v[168:171], v[156:159], v[80:95]\n" \
    "s_waitcnt vmcnt(10)\n" \
    "ds_read_b64_tr_b16 v[160:161], v179 offset:0\n" \
    "ds_read_b64_tr_b16 v[162:163], v179 offset:512\n" \
    "ds_read_b64_tr_b16 v[164:165], v180 offset:0\n" \
    "ds_read_b64_tr_b16 v[166:167], v180 offset:512\n" \
    "v_mfma_f32_32x32x16_bf16 v[96:111], v[172:175], v[152:155], v[96:111]\n" \
    "v_mfma_f32_32x32x16_bf16 v[112:127], v[172:175], v[156:159], v[112:127]\n" \
    "global_load_dwordx4 v[152:155], v178, s[58:59] offset:3072\n" \
    "global_load_dwordx4 v[156:159], v178, s[60:61] offset:3072\n" \
    "s_add_u32 s58, s58, 4096\n" \
    "s_addc_u32 s59, s59, 0\n" \
    "s_add_u32 s60, s60, 4096\n" \
    "s_addc_u32 s61, s61, 0\n" \
    "s_waitcnt lgkmcnt(0)\n" \
    "v_mfma_f32_32x32x16_bf16 v[64:79], v[160:163], v[128:131], v[64:79]\n" \
    "v_mfma_f32_32x32x16_bf16 v[80:95], v[160:163], v[132:135], v[80:95]\n" \
    "s_waitcnt vmcnt(8)\n" \
    "ds_read_b64_tr_b16 v[168:169], v179 offset:2048\n" \
    "ds_read_b64_tr_b16 v[170:171], v179 offset:2560\n" \
    "ds_read_b64_tr_b16 v[172:173], v180 offset:2048\n" \
    "ds_read_b64_tr_b16 v[174:175], v180 offset:2560\n" \
    "v_mfma_f32_32x32x16_bf16 v[96:111], v[164:167], v[128:131], v[96:111]\n" \
    "v_mfma_f32_32x32x16_bf16 v[112:127], v[164:167], v[132:135], v[112:127]\n" \
    "s_waitcnt lgkmcnt(0)\n" \
    "v_mfma_f32_32x32x16_bf16 v[64:79], v[168:171], v[136:139], v[64:79]\n" \
    "v_mfma_f32_32x32x16_bf16 v[80:95], v[168:171], v[140:143], v[80:95]\n" \
    "s_waitcnt vmcnt(4)\n" \
    "ds_read_b64_tr_b16 v[160:161], v179 offset:4096\n" \
    "ds_read_b64_tr_b16 v[162:163], v179 offset:4608\n" \
    "ds_read_b64_tr_b16 v[164:165], v180 offset:4096\n" \
    "ds_read_b64_tr_b16 v[166:167], v180 offset:4608\n" \
    "v_mfma_f32_32x32x16_bf16 v[96:111], v[172:175], v[136:139], v[96:111]\n" \
    "v_mfma_f32_32x32x16_bf16 v[112:127], v[172:175], v[140:143], v[112:127]\n" \
    "s_waitcnt lgkmcnt(0)\n" \
    "v_mfma_f32_32x32x16_bf16 v[64:79], v[160:163], v[144:147], v[64:79]\n" \
    "v_mfma_f32_32x32x16_bf16 v[80:95], v[160:163], v[148:151], v[80:95]\n" \
    "s_waitcnt vmcnt(0)\n" \
    "ds_read_b64_tr_b16 v[168:169], v179 offset:6144\n" \
    "ds_read_b64_tr_b16 v[170:171], v179 offset:6656\n" \
    "ds_read_b64_tr_b16 v[172:173], v180 offset:6144\n" \
    "ds_read_b64_tr_b16 v[174:175], v180 offset:6656\n" \
    "v_mfma_f32_32x32x16_bf16 v[96:111], v[164:167], v[144:147], v[96:111]\n" \
    "v_mfma_f32_32x32x16_bf16 v[112:127], v[164:167], v[148:151], v[112:127]\n" \
    "s_waitcnt lgkmcnt(0)\n" \
    "v_mfma_f32_32x32x16_bf16 v[64:79], v[168:171], v[152:155], v[64:79]\n" \
    "v_mfma_f32_32x32x16_bf16 v[80:95], v[168:171], v[156:159], v[80:95]\n" \
    "v_mfma_f32_32x32x16_bf16 v[96:111], v[172:175], v[152:155], v[96:111]\n" \
    "v_mfma_f32_32x32x16_bf16 v[112:127], v[172:175], v[156:159], v[112:127]\n" \
    "s_nop 15\n" \
    "s_mov_b32 m0, s63\n"
#define G2_PRE \
    "s_mov_b32 s63, m0\n" \
    "s_add_u32 m0, s62, 0\n" \
    "s_nop 0\n" \
    "global_load_lds_dwordx4 v176, s[56:57]\n" \
    "global_load_lds_dwordx4 v177, s[56:57] offset:1024\n" \
    "s_add_u32 s56, s56, 65536\n" \
    "s_addc_u32 s57, s57, 0\n" \
    "s_add_u32 m0, s62, 2048\n" \
    "s_nop 0\n" \
    "global_load_lds_dwordx4 v176, s[56:57]\n" \
    "global_load_lds_dwordx4 v177, s[56:57] offset:1024\n" \
    "s_add_u32 s56, s56, 65536\n" \
    "s_addc_u32 s57, s57, 0\n" \
    "s_add_u32 m0, s62, 4096\n" \
    "s_nop 0\n" \
    "global_load_lds_dwordx4 v176, s[56:57]\n" \
    "global_load_lds_dwordx4 v177, s[56:57] offset:1024\n" \
    "s_add_u32 s56, s56, 65536\n" \
    "s_addc_u32 s57, s57, 0\n" \
    "s_add_u32 m0, s62, 6144\n" \
    "s_nop 0\n" \
    "global_load_lds_dwordx4 v176, s[56:57]\n" \
    "global_load_lds_dwordx4 v177, s[56:57] offset:1024\n" \
    "s_add_u32 s56, s56, 65536\n" \
    "s_addc_u32 s57, s57, 0\n" \
    "s_mov_b32 m0, s63\n"
#define G2_ASM \
    "s_mov_b32 s63, m0\n" \
    "global_load_dwordx4 v[128:131], v178, s[58:59] offset:0 sc1\n" \
    "global_load_dwordx4 v[132:135], v178, s[60:61] offset:0 sc1\n" \
    "global_load_dwordx4 v[136:139], v178, s[58:59] offset:1024 sc1\n" \
    "global_load_dwordx4 v[140:143], v178, s[60:61] offset:1024 sc1\n" \
    "global_load_dwordx4 v[144:147], v178, s[58:59] offset:2048 sc1\n" \
    "global_load_dwordx4 v[148:151], v178, s[60:61] offset:2048 sc1\n" \
    "global_load_dwordx4 v[152:155], v178, s[58:59] offset:3072 sc1\n" \
    "global_load_dwordx4 v[156:159], v178, s[60:61] offset:3072 sc1\n" \
    "s_add_u32 s58, s58, 4096\n" \
    "s_addc_u32 s59, s59, 0\n" \
    "s_add_u32 s60, s60, 4096\n" \
    "s_addc_u32 s61, s61, 0\n" \
    "s_waitcnt vmcnt(6)\n" \
    "ds_read_b64_tr_b16 v[160:161], v179 offset:0\n" \
    "ds_read_b64_tr_b16 v[162:163], v179 offset:1024\n" \
    "ds_read_b64_tr_b16 v[164:165], v180 offset:0\n" \
    "ds_read_b64_tr_b16 v[166:167], v180 offset:1024\n" \
    "s_waitcnt lgkmcnt(0)\n" \
    "v_mfma_f32_32x32x16_bf16 v[64:79], v[128:131], v[160:163], 0\n" \
    "s_add_u32 m0, s62, 0\n" \
    "s_nop 0\n" \
    "global_load_lds_dwordx4 v176, s[56:57]\n" \
    "global_load_lds_dwordx4 v177, s[56:57] offset:1024\n" \
    "s_add_u32 s56, s56, 65536\n" \
    "s_addc_u32 s57, s57, 0\n" \
    "v_mfma_f32_32x32x16_bf16 v[80:95], v[128:131], v[164:167], 0\n" \
    "s_waitcnt vmcnt(6)\n" \
    "ds_read_b64_tr_b16 v[168:169], v179 offset:2048\n" \
    "ds_read_b64_tr_b16 v[170:171], v179 offset:3072\n" \
    "ds_read_b64_tr_b16 v[172:173], v180 offset:2048\n" \
    "ds_read_b64_tr_b16 v[174:175], v180 offset:3072\n" \
    "v_mfma_f32_32x32x16_bf16 v[96:111], v[132:135], v[160:163], 0\n" \
    "v_mfma_f32_32x32x16_bf16 v[112:127], v[132:135], v[164:167], 0\n" \
    "global_load_dwordx4 v[128:131], v178, s[58:59] offset:0 sc1\n" \
    "global_load_dwordx4 v[132:135], v178, s[60:61] offset:0 sc1\n" \
    "s_waitcnt lgkmcnt(0)\n" \
    "v_mfma_f32_32x32x16_bf16 v[64:79], v[136:139], v[168:171], v[64:79]\n" \
    "s_add_u32 m0, s62, 2048\n" \
    "s_nop 0\n" \
    "global_load_lds_dwordx4 v176, s[56:57]\n" \
    "global_load_lds_dwordx4 v177, s[56:57] offset:1024\n" \
    "s_add_u32 s56, s56, 65536\n" \
    "s_addc_u32 s57, s57, 0\n" \
    "v_mfma_f32_32x32x16_bf16 v[80:95], v[136:139], v[172:175], v[80:95]\n" \
    "s_waitcnt vmcnt(8)\n" \
    "ds_read_b64_tr_b16 v[160:161], v179 offset:4096\n" \
    "ds_read_b64_tr_b16 v[162:163], v179 offset:5120\n" \
    "ds_read_b64_tr_b16 v[164:165], v180 offset:4096\n" \
    "ds_read_b64_tr_b16 v[166:167], v180 offset:5120\n" \
    "v_mfma_f32_32x32x16_bf16 v[96:111], v[140:143], v[168:171], v[96:111]\n" \
    "v_mfma_f32_32x32x16_bf16 v[112:127], v[140:143], v[172:175], v[112:127]\n" \
    "global_load_dwordx4 v[136:139], v178, s[58:59] offset:1024 sc1\n" \
    "global_load_dwordx4 v[140:143], v178, s[60:61] offset:1024 sc1\n" \
    "s_waitcnt lgkmcnt(0)\n" \
    "v_mfma_f32_32x32x16_bf16 v[64:79], v[144:147], v[160:163], v[64:79]\n" \
    "s_add_u32 m0, s62, 4096\n" \
    "s_nop 0\n" \
    "global_load_lds_dwordx4 v176, s[56:57]\n" \
    "global_load_lds_dwordx4 v177, s[56:57] offset:1024\n" \
    "s_add_u32 s56, s56, 65536\n" \
    "s_addc_u32 s57, s57, 0\n" \
    "v_mfma_f32_32x32x16_bf16 v[80:95], v[144:147], v[164:167], v[80:95]\n" \
    "s_waitcnt vmcnt(10)\n" \
    "ds_read_b64_tr_b16 v[168:169], v179 offset:6144\n" \
    "ds_read_b64_tr_b16 v[170:171], v179 offset:7168\n" \
    "ds_read_b64_tr_b16 v[172:173], v180 offset:6144\n" \
    "ds_read_b64_tr_b16 v[174:175], v180 offset:7168\n" \
    "v_mfma_f32_32x32x16_bf16 v[96:111], v[148:151], v[160:163], v[96:111]\n" \
    "v_mfma_f32_32x32x16_bf16 v[112:127], v[148:151], v[164:167], v[112:127]\n" \
    "global_load_dwordx4 v[144:147], v178, s[58:59] offset:2048 sc1\n" \
    "global_load_dwordx4 v[148:151], v178, s[60:61] offset:2048 sc1\n" \
    "s_waitcnt lgkmcnt(0)\n" \
    "v_mfma_f32_32x32x16_bf16 v[64:79], v[152:155], v[168:171], v[64:79]\n" \
    "s_add_u32 m0, s62, 6144\n" \
    "s_nop 0\n" \
    "global_load_lds_dwordx4 v176, s[56:57]\n" \
    "global_load_lds_dwordx4 v177, s[56:57] offset:1024\n" \
    "s_add_u32 s56, s56, 65536\n" \
    "s_addc_u32 s57, s57, 0\n" \
    "v_mfma_f32_32x32x16_bf16 v[80:95], v[152:155], v[172:175], v[80:95]\n" \
    "s_waitcnt vmcnt(10)\n" \
    "ds_read_b64_tr_b16 v[160:161], v179 offset:0\n" \
    "ds_read_b64_tr_b16 v[162:163], v179 offset:1024\n" \
    "ds_read_b64_tr_b16 v[164:165], v180 offset:0\n" \
    "ds_read_b64_tr_b16 v[166:167], v180 offset:1024\n" \
    "v_mfma_f32_32x32x16_bf16 v[96:111], v[156:159], v[168:171], v[96:111]\n" \
    "v_mfma_f32_32x32x16_bf16 v[112:127], v[156:159], v[172:175], v[112:127]\n" \
    "global_load_dwordx4 v[152:155], v178, s[58:59] offset:3072 sc1\n" \
    "global_load_dwordx4 v[156:159], v178, s[60:61] offset:3072 sc1\n" \
    "s_add_u32 s58, s58, 4096\n" \
    "s_addc_u32 s59, s59, 0\n" \
    "s_add_u32 s60, s60, 4096\n" \
    "s_addc_u32 s61, s61, 0\n" \
    "s_waitcnt lgkmcnt(0)\n" \
    "v_mfma_f32_32x32x16_bf16 v[64:79], v[128:131], v[160:163], v[64:79]\n" \
    "s_add_u32 m0, s62, 0\n" \
    "s_nop 0\n" \
    "global_load_lds_dwordx4 v176, s[56:57]\n" \
    "global_load_lds_dwordx4 v177, s[56:57] offset:1024\n" \
    "s_add_u32 s56, s56, 65536\n" \
    "s_addc_u32 s57, s57, 0\n" \
    "v_mfma_f32_32x32x16_bf16 v[80:95], v[128:131], v[164:167], v[80:95]\n" \
    "s_waitcnt vmcnt(10)\n" \
    "ds_read_b64_tr_b16 v[168:169], v179 offset:2048\n" \
    "ds_read_b64_tr_b16 v[170:171], v179 offset:3072\n" \
    "ds_read_b64_tr_b16 v[172:173], v180 offset:2048\n" \
    "ds_read_b64_tr_b16 v[174:175], v180 offset:3072\n" \
    "v_mfma_f32_32x32x16_bf16 v[96:111], v[132:135], v[160:163], v[96:111]\n" \
    "v_mfma_f32_32x32x16_bf16 v[112:127], v[132:135], v[164:167], v[112:127]\n" \
    "global_load_dwordx4 v[128:131], v178, s[58:59] offset:0 sc1\n" \
    "global_load_dwordx4 v[132:135], v178, s[60:61] offset:0 sc1\n" \
    "s_waitcnt lgkmcnt(0)\n" \
    "v_mfma_f32_32x32x16_bf16 v[64:79], v[136:139], v[168:171], v[64:79]\n" \
    "s_add_u32 m0, s62, 2048\n" \
    "s_nop 0\n" \
    "global_load_lds_dwordx4 v176, s[56:57]\n" \
    "global_load_lds_dwordx4 v177, s[56:57] offset:1024\n" \
    "s_add_u32 s56, s56, 65536\n" \
    "s_addc_u32 s57, s57, 0\n" \
    "v_mfma_f32_32x32x16_bf16 v[80:95], v[136:139], v[172:175], v[80:95]\n" \
    "s_waitcnt vmcnt(10)\n" \
    "ds_read_b64_tr_b16 v[160:161], v179 offset:4096\n" \
    "ds_read_b64_tr_b16 v[162:163], v179 offset:5120\n" \
    "ds_read_b64_tr_b16 v[164:165], v180 offset:4096\n" \
    "ds_read_b64_tr_b16 v[166:167], v180 offset:5120\n" \
    "v_mfma_f32_32x32x16_bf16 v[96:111], v[140:143], v[168:171], v[96:111]\n" \
    "v_mfma_f32_32x32x16_bf16 v[112:127], v[140:143], v[172:175], v[112:127]\n" \
    "global_load_dwordx4 v[136:139], v178, s[58:59] offset:1024 sc1\n" \
    "global_load_dwordx4 v[140:143], v178, s[60:61] offset:1024 sc1\n" \
    "s_waitcnt lgkmcnt(0)\n" \
    "v_mfma_f32_32x32x16_bf16 v[64:79], v[144:147], v[160:163], v[64:79]\n" \
    "s_add_u32 m0, s62, 4096\n" \
    "s_nop 0\n" \
    "global_load_lds_dwordx4 v176, s[56:57]\n" \
    "global_load_lds_dwordx4 v177, s[56:57] offset:1024\n" \
    "s_add_u32 s56, s56, 65536\n" \
    "s_addc_u32 s57, s57, 0\n" \
    "v_mfma_f32_32x32x16_bf16 v[80:95], v[144:147], v[164:167], v[80:95]\n" \
    "s_waitcnt vmcnt(10)\n" \
    "ds_read_b64_tr_b16 v[168:169], v179 offset:6144\n" \
    "ds_read_b64_tr_b16 v[170:171], v179 offset:7168\n" \
    "ds_read_b64_tr_b16 v[172:173], v180 offset:6144\n" \
    "ds_read_b64_tr_b16 v[174:175], v180 offset:7168\n" \
    "v_mfma_f32_32x32x16_bf16 v[96:111], v[148:151], v[160:163], v[96:111]\n" \
    "v_mfma_f32_32x32x16_bf16 v[112:127], v[148:151], v[164:167], v[112:127]\n" \
    "global_load_dwordx4 v[144:147], v178, s[58:59] offset:2048 sc1\n" \
    "global_load_dwordx4 v[148:151], v178, s[60:61] offset:2048 sc1\n" \
    "s_waitcnt lgkmcnt(0)\n" \
    "v_mfma_f32_32x32x16_bf16 v[64:79], v[152:155], v[168:171], v[64:79]\n" \
    "s_add_u32 m0, s62, 6144\n" \
    "s_nop 0\n" \
    "global_load_lds_dwordx4 v176, s[56:57]\n" \
    "global_load_lds_dwordx4 v177, s[56:57] offset:1024\n" \
    "s_add_u32 s56, s56, 65536\n" \
    "s_addc_u32 s57, s57, 0\n" \
    "v_mfma_f32_32x32x16_bf16 v[80:95], v[152:155], v[172:175], v[80:95]\n" \
    "s_waitcnt vmcnt(10)\n" \
    "ds_read_b64_tr_b16 v[160:161], v179 offset:0\n" \
    "ds_read_b64_tr_b16 v[162:163], v179 offset:1024\n" \
    "ds_read_b64_tr_b16 v[164:165], v180 offset:0\n" \
    "ds_read_b64_tr_b16 v[166:167], v180 offset:1024\n" \
    "v_mfma_f32_32x32x16_bf16 v[96:111], v[156:159], v[168:171], v[96:111]\n" \
    "v_mfma_f32_32x32x16_bf16 v[112:127], v[156:159], v[172:175], v[112:127]\n" \
    "global_load_dwordx4 v[152:155], v178, s[58:59] offset:3072 sc1\n" \
    "global_load_dwordx4 v[156:159], v178, s[60:61] offset:3072 sc1\n" \
    "s_add_u32 s58, s58, 4096\n" \
    "s_addc_u32 s59, s59, 0\n" \
    "s_add_u32 s60, s60, 4096\n" \
    "s_addc_u32 s61, s61, 0\n" \
    "s_waitcnt lgkmcnt(0)\n" \
    "v_mfma_f32_32x32x16_bf16 v[64:79], v[128:131], v[160:163], v[64:79]\n" \
    "s_add_u32 m0, s62, 0\n" \
    "s_nop 0\n" \
    "global_load_lds_dwordx4 v176, s[56:57]\n" \
    "global_load_lds_dwordx4 v177, s[56:57] offset:1024\n" \
    "s_add_u32 s56, s56, 65536\n" \
    "s_addc_u32 s57, s57, 0\n" \
    "v_mfma_f32_32x32x16_bf16 v[80:95], v[128:131], v[164:167], v[80:95]\n" \
    "s_waitcnt vmcnt(10)\n" \
    "ds_read_b64_tr_b16 v[168:169], v179 offset:2048\n" \
    "ds_read_b64_tr_b16 v[170:171], v179 offset:3072\n" \
    "ds_read_b64_tr_b16 v[172:173], v180 offset:2048\n" \
    "ds_read_b64_tr_b16 v[174:175], v180 offset:3072\n" \
    "v_mfma_f32_32x32x16_bf16 v[96:111], v[132:135], v[160:163], v[96:111]\n" \
    "v_mfma_f32_32x32x16_bf16 v[112:127], v[132:135], v[164:167], v[112:127]\n" \
    "global_load_dwordx4 v[128:131], v178, s[58:59] offset:0 sc1\n" \
    "global_load_dwordx4 v[132:135], v178, s[60:61] offset:0 sc1\n" \
    "s_waitcnt lgkmcnt(0)\n" \
    "v_mfma_f32_32x32x16_bf16 v[64:79], v[136:139], v[168:171], v[64:79]\n" \
    "s_add_u32 m0, s62, 2048\n" \
    "s_nop 0\n" \
    "global_load_lds_dwordx4 v176, s[56:57]\n" \
    "global_load_lds_dwordx4 v177, s[56:57] offset:1024\n" \
    "s_add_u32 s56, s56, 65536\n" \
    "s_addc_u32 s57, s57, 0\n" \
    "v_mfma_f32_32x32x16_bf16 v[80:95], v[136:139], v[172:175], v[80:95]\n" \
    "s_waitcnt vmcnt(10)\n" \
    "ds_read_b64_tr_b16 v[160:161], v179 offset:4096\n" \
    "ds_read_b64_tr_b16 v[162:163], v179 offset:5120\n" \
    "ds_read_b64_tr_b16 v[164:165], v180 offset:4096\n" \
    "ds_read_b64_tr_b16 v[166:167], v180 offset:5120\n" \
    "v_mfma_f32_32x32x16_bf16 v[96:111], v[140:143], v[168:171], v[96:111]\n" \
    "v_mfma_f32_32x32x16_bf16 v[112:127], v[140:143], v[172:175], v[112:127]\n" \
    "global_load_dwordx4 v[136:139], v178, s[58:59] offset:1024 sc1\n" \
    "global_load_dwordx4 v[140:143], v178, s[60:61] offset:1024 sc1\n" \
    "s_waitcnt lgkmcnt(0)\n" \
    "v_mfma_f32_32x32x16_bf16 v[64:79], v[144:147], v[160:163], v[64:79]\n" \
    "s_add_u32 m0, s62, 4096\n" \
    "s_nop 0\n" \
    "global_load_lds_dwordx4 v176, s[56:57]\n" \
    "global_load_lds_dwordx4 v177, s[56:57] offset:1024\n" \
    "s_add_u32 s56, s56, 65536\n" \
    "s_addc_u32 s57, s57, 0\n" \
    "v_mfma_f32_32x32x16_bf16 v[80:95], v[144:147], v[164:167], v[80:95]\n" \
    "s_waitcnt vmcnt(10)\n" \
    "ds_read_b64_tr_b16 v[168:169], v179 offset:6144\n" \
    "ds_read_b64_tr_b16 v[170:171], v179 offset:7168\n" \
    "ds_read_b64_tr_b16 v[172:173], v180 offset:6144\n" \
    "ds_read_b64_tr_b16 v[174:175], v180 offset:7168\n" \
    "v_mfma_f32_32x32x16_bf16 v[96:111], v[148:151], v[160:163], v[96:111]\n" \
    "v_mfma_f32_32x32x16_bf16 v[112:127], v[148:151], v[164:167], v[112:127]\n" \
    "global_load_dwordx4 v[144:147], v178, s[58:59] offset:2048 sc1\n" \
    "global_load_dwordx4 v[148:151], v178, s[60:61] offset:2048 sc1\n" \
    "s_waitcnt lgkmcnt(0)\n" \
    "v_mfma_f32_32x32x16_bf16 v[64:79], v[152:155], v[168:171], v[64:79]\n" \
    "s_add_u32 m0, s62, 6144\n" \
    "s_nop 0\n" \
    "global_load_lds_dwordx4 v176, s[56:57]\n" \
    "global_load_lds_dwordx4 v177, s[56:57] offset:1024\n" \
    "s_add_u32 s56, s56, 65536\n" \
    "s_addc_u32 s57, s57, 0\n" \
    "v_mfma_f32_32x32x16_bf16 v[80:95], v[152:155], v[172:175], v[80:95]\n" \
    "s_waitcnt vmcnt(10)\n" \
    "ds_read_b64_tr_b16 v[160:161], v179 offset:0\n" \
    "ds_read_b64_tr_b16 v[162:163], v179 offset:1024\n" \
    "ds_read_b64_tr_b16 v[164:165], v180 offset:0\n" \
    "ds_read_b64_tr_b16 v[166:167], v180 offset:1024\n" \
    "v_mfma_f32_32x32x16_bf16 v[96:111], v[156:159], v[168:171], v[96:111]\n" \
    "v_mfma_f32_32x32x16_bf16 v[112:127], v[156:159], v[172:175], v[112:127]\n" \
    "global_load_dwordx4 v[152:155], v178, s[58:59] offset:3072 sc1\n" \
    "global_load_dwordx4 v[156:159], v178, s[60:61] offset:3072 sc1\n" \
    "s_add_u32 s58, s58, 4096\n" \
    "s_addc_u32 s59, s59, 0\n" \
    "s_add_u32 s60, s60, 4096\n" \
    "s_addc_u32 s61, s61, 0\n" \
    "s_waitcnt lgkmcnt(0)\n" \
    "v_mfma_f32_32x32x16_bf16 v[64:79], v[128:131], v[160:163], v[64:79]\n" \
    "s_add_u32 m0, s62, 0\n" \
    "s_nop 0\n" \
    "global_load_lds_dwordx4 v176, s[56:57]\n" \
    "global_load_lds_dwordx4 v177, s[56:57] offset:1024\n" \
    "s_add_u32 s56, s56, 65536\n" \
    "s_addc_u32 s57, s57, 0\n" \
    "v_mfma_f32_32x32x16_bf16 v[80:95], v[128:131], v[164:167], v[80:95]\n" \
    "s_waitcnt vmcnt(10)\n" \
    "ds_read_b64_tr_b16 v[168:169], v179 offset:2048\n" \
    "ds_read_b64_tr_b16 v[170:171], v179 offset:3072\n" \
    "ds_read_b64_tr_b16 v[172:173], v180 offset:2048\n" \
    "ds_read_b64_tr_b16 v[174:175], v180 offset:3072\n" \
    "v_mfma_f32_32x32x16_bf16 v[96:111], v[132:135], v[160:163], v[96:111]\n" \
    "v_mfma_f32_32x32x16_bf16 v[112:127], v[132:135], v[164:167], v[112:127]\n" \
    "global_load_dwordx4 v[128:131], v178, s[58:59] offset:0 sc1\n" \
    "global_load_dwordx4 v[132:135], v178, s[60:61] offset:0 sc1\n" \
    "s_waitcnt lgkmcnt(0)\n" \
    "v_mfma_f32_32x32x16_bf16 v[64:79], v[136:139], v[168:171], v[64:79]\n" \
    "s_add_u32 m0, s62, 2048\n" \
    "s_nop 0\n" \
    "global_load_lds_dwordx4 v176, s[56:57]\n" \
    "global_load_lds_dwordx4 v177, s[56:57] offset:1024\n" \
    "s_add_u32 s56, s56, 65536\n" \
    "s_addc_u32 s57, s57, 0\n" \
    "v_mfma_f32_32x32x16_bf16 v[80:95], v[136:139], v[172:175], v[80:95]\n" \
    "s_waitcnt vmcnt(10)\n" \
    "ds_read_b64_tr_b16 v[160:161], v179 offset:4096\n" \
    "ds_read_b64_tr_b16 v[162:163], v179 offset:5120\n" \
    "ds_read_b64_tr_b16 v[164:165], v180 offset:4096\n" \
    "ds_read_b64_tr_b16 v[166:167], v180 offset:5120\n" \
    "v_mfma_f32_32x32x16_bf16 v[96:111], v[140:143], v[168:171], v[96:111]\n" \
    "v_mfma_f32_32x32x16_bf16 v[112:127], v[140:143], v[172:175], v[112:127]\n" \
    "global_load_dwordx4 v[136:139], v178, s[58:59] offset:1024 sc1\n" \
    "global_load_dwordx4 v[140:143], v178, s[60:61] offset:1024 sc1\n" \
    "s_waitcnt lgkmcnt(0)\n" \
    "v_mfma_f32_32x32x16_bf16 v[64:79], v[144:147], v[160:163], v[64:79]\n" \
    "s_add_u32 m0, s62, 4096\n" \
    "s_nop 0\n" \
    "global_load_lds_dwordx4 v176, s[56:57]\n" \
    "global_load_lds_dwordx4 v177, s[56:57] offset:1024\n" \
    "s_add_u32 s56, s56, 65536\n" \
    "s_addc_u32 s57, s57, 0\n" \
    "v_mfma_f32_32x32x16_bf16 v[80:95], v[144:147], v[164:167], v[80:95]\n" \
    "s_waitcnt vmcnt(10)\n" \
    "ds_read_b64_tr_b16 v[168:169], v179 offset:6144\n" \
    "ds_read_b64_tr_b16 v[170:171], v179 offset:7168\n" \
    "ds_read_b64_tr_b16 v[172:173], v180 offset:6144\n" \
    "ds_read_b64_tr_b16 v[174:175], v180 offset:7168\n" \
    "v_mfma_f32_32x32x16_bf16 v[96:111], v[148:151], v[160:163], v[96:111]\n" \
    "v_mfma_f32_32x32x16_bf16 v[112:127], v[148:151], v[164:167], v[112:127]\n" \
    "global_load_dwordx4 v[144:147], v178, s[58:59] offset:2048 sc1\n" \
    "global_load_dwordx4 v[148:151], v178, s[60:61] offset:2048 sc1\n" \
    "s_waitcnt lgkmcnt(0)\n" \
    "v_mfma_f32_32x32x16_bf16 v[64:79], v[152:155], v[168:171], v[64:79]\n" \
    "s_add_u32 m0, s62, 6144\n" \
    "s_nop 0\n" \
    "global_load_lds_dwordx4 v176, s[56:57]\n" \
    "global_load_lds_dwordx4 v177, s[56:57] offset:1024\n" \
    "s_add_u32 s56, s56, 65536\n" \
    "s_addc_u32 s57, s57, 0\n" \
    "v_mfma_f32_32x32x16_bf16 v[80:95], v[152:155], v[172:175], v[80:95]\n" \
    "s_waitcnt vmcnt(10)\n" \
    "ds_read_b64_tr_b16 v[160:161], v179 offset:0\n" \
    "ds_read_b64_tr_b16 v[162:163], v179 offset:1024\n" \
    "ds_read_b64_tr_b16 v[164:165], v180 offset:0\n" \
    "ds_read_b64_tr_b16 v[166:167], v180 offset:1024\n" \
    "v_mfma_f32_32x32x16_bf16 v[96:111], v[156:159], v[168:171], v[96:111]\n" \
    "v_mfma_f32_32x32x16_bf16 v[112:127], v[156:159], v[172:175], v[112:127]\n" \
    "global_load_dwordx4 v[152:155], v178, s[58:59] offset:3072 sc1\n" \
    "global_load_dwordx4 v[156:159], v178, s[60:61] offset:3072 sc1\n" \
    "s_add_u32 s58, s58, 4096\n" \
    "s_addc_u32 s59, s59, 0\n" \
    "s_add_u32 s60, s60, 4096\n" \
    "s_addc_u32 s61, s61, 0\n" \
    "s_waitcnt lgkmcnt(0)\n" \
    "v_mfma_f32_32x32x16_bf16 v[64:79], v[128:131], v[160:163], v[64:79]\n" \
    "s_add_u32 m0, s62, 0\n" \
    "s_nop 0\n" \
    "global_load_lds_dwordx4 v176, s[56:57]\n" \
    "global_load_lds_dwordx4 v177, s[56:57] offset:1024\n" \
    "s_add_u32 s56, s56, 65536\n" \
    "s_addc_u32 s57, s57, 0\n" \
    "v_mfma_f32_32x32x16_bf16 v[80:95], v[128:131], v[164:167], v[80:95]\n" \
    "s_waitcnt vmcnt(10)\n" \
    "ds_read_b64_tr_b16 v[168:169], v179 offset:2048\n" \
    "ds_read_b64_tr_b16 v[170:171], v179 offset:3072\n" \
    "ds_read_b64_tr_b16 v[172:173], v180 offset:2048\n" \
    "ds_read_b64_tr_b16 v[174:175], v180 offset:3072\n" \
    "v_mfma_f32_32x32x16_bf16 v[96:111], v[132:135], v[160:163], v[96:111]\n" \
    "v_mfma_f32_32x32x16_bf16 v[112:127], v[132:135], v[164:167], v[112:127]\n" \
    "global_load_dwordx4 v[128:131], v178, s[58:59] offset:0 sc1\n" \
    "global_load_dwordx4 v[132:135], v178, s[60:61] offset:0 sc1\n" \
    "s_waitcnt lgkmcnt(0)\n" \
    "v_mfma_f32_32x32x16_bf16 v[64:79], v[136:139], v[168:171], v[64:79]\n" \
    "s_add_u32 m0, s62, 2048\n" \
    "s_nop 0\n" \
    "global_load_lds_dwordx4 v176, s[56:57]\n" \
    "global_load_lds_dwordx4 v177, s[56:57] offset:1024\n" \
    "s_add_u32 s56, s56, 65536\n" \
    "s_addc_u32 s57, s57, 0\n" \
    "v_mfma_f32_32x32x16_bf16 v[80:95], v[136:139], v[172:175], v[80:95]\n" \
    "s_waitcnt vmcnt(10)\n" \
    "ds_read_b64_tr_b16 v[160:161], v179 offset:4096\n" \
    "ds_read_b64_tr_b16 v[162:163], v179 offset:5120\n" \
    "ds_read_b64_tr_b16 v[164:165], v180 offset:4096\n" \
    "ds_read_b64_tr_b16 v[166:167], v180 offset:5120\n" \
    "v_mfma_f32_32x32x16_bf16 v[96:111], v[140:143], v[168:171], v[96:111]\n" \
    "v_mfma_f32_32x32x16_bf16 v[112:127], v[140:143], v[172:175], v[112:127]\n" \
    "global_load_dwordx4 v[136:139], v178, s[58:59] offset:1024 sc1\n" \
    "global_load_dwordx4 v[140:143], v178, s[60:61] offset:1024 sc1\n" \
    "s_waitcnt lgkmcnt(0)\n" \
    "v_mfma_f32_32x32x16_bf16 v[64:79], v[144:147], v[160:163], v[64:79]\n" \
    "s_add_u32 m0, s62, 4096\n" \
    "s_nop 0\n" \
    "global_load_lds_dwordx4 v176, s[56:57]\n" \
    "global_load_lds_dwordx4 v177, s[56:57] offset:1024\n" \
    "s_add_u32 s56, s56, 65536\n" \
    "s_addc_u32 s57, s57, 0\n" \
    "v_mfma_f32_32x32x16_bf16 v[80:95], v[144:147], v[164:167], v[80:95]\n" \
    "s_waitcnt vmcnt(10)\n" \
    "ds_read_b64_tr_b16 v[168:169], v179 offset:6144\n" \
    "ds_read_b64_tr_b16 v[170:171], v179 offset:7168\n" \
    "ds_read_b64_tr_b16 v[172:173], v180 offset:6144\n" \
    "ds_read_b64_tr_b16 v[174:175], v180 offset:7168\n" \
    "v_mfma_f32_32x32x16_bf16 v[96:111], v[148:151], v[160:163], v[96:111]\n" \
    "v_mfma_f32_32x32x16_bf16 v[112:127], v[148:151], v[164:167], v[112:127]\n" \
    "global_load_dwordx4 v[144:147], v178, s[58:59] offset:2048 sc1\n" \
    "global_load_dwordx4 v[148:151], v178, s[60:61] offset:2048 sc1\n" \
    "s_waitcnt lgkmcnt(0)\n" \
    "v_mfma_f32_32x32x16_bf16 v[64:79], v[152:155], v[168:171], v[64:79]\n" \
    "s_add_u32 m0, s62, 6144\n" \
    "s_nop 0\n" \
    "global_load_lds_dwordx4 v176, s[56:57]\n" \
    "global_load_lds_dwordx4 v177, s[56:57] offset:1024\n" \
    "s_add_u32 s56, s56, 65536\n" \
    "s_addc_u32 s57, s57, 0\n" \
    "v_mfma_f32_32x32x16_bf16 v[80:95], v[152:155], v[172:175], v[80:95]\n" \
    "s_waitcnt vmcnt(10)\n" \
    "ds_read_b64_tr_b16 v[160:161], v179 offset:0\n" \
    "ds_read_b64_tr_b16 v[162:163], v179 offset:1024\n" \
    "ds_read_b64_tr_b16 v[164:165], v180 offset:0\n" \
    "ds_read_b64_tr_b16 v[166:167], v180 offset:1024\n" \
    "v_mfma_f32_32x32x16_bf16 v[96:111], v[156:159], v[168:171], v[96:111]\n" \
    "v_mfma_f32_32x32x16_bf16 v[112:127], v[156:159], v[172:175], v[112:127]\n" \
    "global_load_dwordx4 v[152:155], v178, s[58:59] offset:3072 sc1\n" \
    "global_load_dwordx4 v[156:159], v178, s[60:61] offset:3072 sc1\n" \
    "s_add_u32 s58, s58, 4096\n" \
    "s_addc_u32 s59, s59, 0\n" \
    "s_add_u32 s60, s60, 4096\n" \
    "s_addc_u32 s61, s61, 0\n" \
    "s_waitcnt lgkmcnt(0)\n" \
    "v_mfma_f32_32x32x16_bf16 v[64:79], v[128:131], v[160:163], v[64:79]\n" \
    "s_add_u32 m0, s62, 0\n" \
    "s_nop 0\n" \
    "global_load_lds_dwordx4 v176, s[56:57]\n" \
    "global_load_lds_dwordx4 v177, s[56:57] offset:1024\n" \
    "s_add_u32 s56, s56, 65536\n" \
    "s_addc_u32 s57, s57, 0\n" \
    "v_mfma_f32_32x32x16_bf16 v[80:95], v[128:131], v[164:167], v[80:95]\n" \
    "s_waitcnt vmcnt(10)\n" \
    "ds_read_b64_tr_b16 v[168:169], v179 offset:2048\n" \
    "ds_read_b64_tr_b16 v[170:171], v179 offset:3072\n" \
    "ds_read_b64_tr_b16 v[172:173], v180 offset:2048\n" \
    "ds_read_b64_tr_b16 v[174:175], v180 offset:3072\n" \
    "v_mfma_f32_32x32x16_bf16 v[96:111], v[132:135], v[160:163], v[96:111]\n" \
    "v_mfma_f32_32x32x16_bf16 v[112:127], v[132:135], v[164:167], v[112:127]\n" \
    "global_load_dwordx4 v[128:131], v178, s[58:59] offset:0 sc1\n" \
    "global_load_dwordx4 v[132:135], v178, s[60:61] offset:0 sc1\n" \
    "s_waitcnt lgkmcnt(0)\n" \
    "v_mfma_f32_32x32x16_bf16 v[64:79], v[136:139], v[168:171], v[64:79]\n" \
    "s_add_u32 m0, s62, 2048\n" \
    "s_nop 0\n" \
    "global_load_lds_dwordx4 v176, s[56:57]\n" \
    "global_load_lds_dwordx4 v177, s[56:57] offset:1024\n" \
    "s_add_u32 s56, s56, 65536\n" \
    "s_addc_u32 s57, s57, 0\n" \
    "v_mfma_f32_32x32x16_bf16 v[80:95], v[136:139], v[172:175], v[80:95]\n" \
    "s_waitcnt vmcnt(10)\n" \
    "ds_read_b64_tr_b16 v[160:161], v179 offset:4096\n" \
    "ds_read_b64_tr_b16 v[162:163], v179 offset:5120\n" \
    "ds_read_b64_tr_b16 v[164:165], v180 offset:4096\n" \
    "ds_read_b64_tr_b16 v[166:167], v180 offset:5120\n" \
    "v_mfma_f32_32x32x16_bf16 v[96:111], v[140:143], v[168:171], v[96:111]\n" \
    "v_mfma_f32_32x32x16_bf16 v[112:127], v[140:143], v[172:175], v[112:127]\n" \
    "global_load_dwordx4 v[136:139], v178, s[58:59] offset:1024 sc1\n" \
    "global_load_dwordx4 v[140:143], v178, s[60:61] offset:1024 sc1\n" \
    "s_waitcnt lgkmcnt(0)\n" \
    "v_mfma_f32_32x32x16_bf16 v[64:79], v[144:147], v[160:163], v[64:79]\n" \
    "s_add_u32 m0, s62, 4096\n" \
    "s_nop 0\n" \
    "global_load_lds_dwordx4 v176, s[56:57]\n" \
    "global_load_lds_dwordx4 v177, s[56:57] offset:1024\n" \
    "s_add_u32 s56, s56, 65536\n" \
    "s_addc_u32 s57, s57, 0\n" \
    "v_mfma_f32_32x32x16_bf16 v[80:95], v[144:147], v[164:167], v[80:95]\n" \
    "s_waitcnt vmcnt(10)\n" \
    "ds_read_b64_tr_b16 v[168:169], v179 offset:6144\n" \
    "ds_read_b64_tr_b16 v[170:171], v179 offset:7168\n" \
    "ds_read_b64_tr_b16 v[172:173], v180 offset:6144\n" \
    "ds_read_b64_tr_b16 v[174:175], v180 offset:7168\n" \
    "v_mfma_f32_32x32x16_bf16 v[96:111], v[148:151], v[160:163], v[96:111]\n" \
    "v_mfma_f32_32x32x16_bf16 v[112:127], v[148:151], v[164:167], v[112:127]\n" \
    "global_load_dwordx4 v[144:147], v178, s[58:59] offset:2048 sc1\n" \
    "global_load_dwordx4 v[148:151], v178, s[60:61] offset:2048 sc1\n" \
    "s_waitcnt lgkmcnt(0)\n" \
    "v_mfma_f32_32x32x16_bf16 v[64:79], v[152:155], v[168:171], v[64:79]\n" \
    "s_add_u32 m0, s62, 6144\n" \
    "s_nop 0\n" \
    "global_load_lds_dwordx4 v176, s[56:57]\n" \
    "global_load_lds_dwordx4 v177, s[56:57] offset:1024\n" \
    "s_add_u32 s56, s56, 65536\n" \
    "s_addc_u32 s57, s57, 0\n" \
    "v_mfma_f32_32x32x16_bf16 v[80:95], v[152:155], v[172:175], v[80:95]\n" \
    "s_waitcnt vmcnt(10)\n" \
    "ds_read_b64_tr_b16 v[160:161], v179 offset:0\n" \
    "ds_read_b64_tr_b16 v[162:163], v179 offset:1024\n" \
    "ds_read_b64_tr_b16 v[164:165], v180 offset:0\n" \
    "ds_read_b64_tr_b16 v[166:167], v180 offset:1024\n" \
    "v_mfma_f32_32x32x16_bf16 v[96:111], v[156:159], v[168:171], v[96:111]\n" \
    "v_mfma_f32_32x32x16_bf16 v[112:127], v[156:159], v[172:175], v[112:127]\n" \
    "global_load_dwordx4 v[152:155], v178, s[58:59] offset:3072 sc1\n" \
    "global_load_dwordx4 v[156:159], v178, s[60:61] offset:3072 sc1\n" \
    "s_add_u32 s58, s58, 4096\n" \
    "s_addc_u32 s59, s59, 0\n" \
    "s_add_u32 s60, s60, 4096\n" \
    "s_addc_u32 s61, s61, 0\n" \
    "s_waitcnt lgkmcnt(0)\n" \
    "v_mfma_f32_32x32x16_bf16 v[64:79], v[128:131], v[160:163], v[64:79]\n" \
    "v_mfma_f32_32x32x16_bf16 v[80:95], v[128:131], v[164:167], v[80:95]\n" \
    "s_waitcnt vmcnt(8)\n" \
    "ds_read_b64_tr_b16 v[168:169], v179 offset:2048\n" \
    "ds_read_b64_tr_b16 v[170:171], v179 offset:3072\n" \
    "ds_read_b64_tr_b16 v[172:173], v180 offset:2048\n" \
    "ds_read_b64_tr_b16 v[174:175], v180 offset:3072\n" \
    "v_mfma_f32_32x32x16_bf16 v[96:111], v[132:135], v[160:163], v[96:111]\n" \
    "v_mfma_f32_32x32x16_bf16 v[112:127], v[132:135], v[164:167], v[112:127]\n" \
    "s_waitcnt lgkmcnt(0)\n" \
    "v_mfma_f32_32x32x16_bf16 v[64:79], v[136:139], v[168:171], v[64:79]\n" \
    "v_mfma_f32_32x32x16_bf16 v[80:95], v[136:139], v[172:175], v[80:95]\n" \
    "s_waitcnt vmcnt(4)\n" \
    "ds_read_b64_tr_b16 v[160:161], v179 offset:4096\n" \
    "ds_read_b64_tr_b16 v[162:163], v179 offset:5120\n" \
    "ds_read_b64_tr_b16 v[164:165], v180 offset:4096\n" \
    "ds_read_b64_tr_b16 v[166:167], v180 offset:5120\n" \
    "v_mfma_f32_32x32x16_bf16 v[96:111], v[140:143], v[168:171], v[96:111]\n" \
    "v_mfma_f32_32x32x16_bf16 v[112:127], v[140:143], v[172:175], v[112:127]\n" \
    "s_waitcnt lgkmcnt(0)\n" \
    "v_mfma_f32_32x32x16_bf16 v[64:79], v[144:147], v[160:163], v[64:79]\n" \
    "v_mfma_f32_32x32x16_bf16 v[80:95], v[144:147], v[164:167], v[80:95]\n" \
    "s_waitcnt vmcnt(0)\n" \
    "ds_read_b64_tr_b16 v[168:169], v179 offset:6144\n" \
    "ds_read_b64_tr_b16 v[170:171], v179 offset:7168\n" \
    "ds_read_b64_tr_b16 v[172:173], v180 offset:6144\n" \
    "ds_read_b64_tr_b16 v[174:175], v180 offset:7168\n" \
    "v_mfma_f32_32x32x16_bf16 v[96:111], v[148:151], v[160:163], v[96:111]\n" \
    "v_mfma_f32_32x32x16_bf16 v[112:127], v[148:151], v[164:167], v[112:127]\n" \
    "s_waitcnt lgkmcnt(0)\n" \
    "v_mfma_f32_32x32x16_bf16 v[64:79], v[152:155], v[168:171], v[64:79]\n" \
    "v_mfma_f32_32x32x16_bf16 v[80:95], v[152:155], v[172:175], v[80:95]\n" \
    "v_mfma_f32_32x32x16_bf16 v[96:111], v[156:159], v[168:171], v[96:111]\n" \
    "v_mfma_f32_32x32x16_bf16 v[112:127], v[156:159], v[172:175], v[112:127]\n" \
    "s_nop 15\n" \
    "s_mov_b32 m0, s63\n"
#define G3_PRE \
    "s_mov_b32 s63, m0\n" \
    "s_add_u32 m0, s62, 0\n" \
    "s_nop 0\n" \
    "global_load_lds_dwordx4 v224, s[56:57]\n" \
    "global_load_lds_dwordx4 v225, s[56:57] offset:1024\n" \
    "s_add_u32 s56, s56, 16384\n" \
    "s_addc_u32 s57, s57, 0\n" \
    "s_add_u32 m0, s62, 2048\n" \
    "s_nop 0\n" \
    "global_load_lds_dwordx4 v224, s[56:57]\n" \
    "global_load_lds_dwordx4 v225, s[56:57] offset:1024\n" \
    "s_add_u32 s56, s56, 16384\n" \
    "s_addc_u32 s57, s57, 0\n" \
    "s_add_u32 m0, s62, 4096\n" \
    "s_nop 0\n" \
    "global_load_lds_dwordx4 v224, s[56:57]\n" \
    "global_load_lds_dwordx4 v225, s[56:57] offset:1024\n" \
    "s_add_u32 s56, s56, 16384\n" \
    "s_addc_u32 s57, s57, 0\n" \
    "s_add_u32 m0, s62, 6144\n" \
    "s_nop 0\n" \
    "global_load_lds_dwordx4 v224, s[56:57]\n" \
    "global_load_lds_dwordx4 v225, s[56:57] offset:1024\n" \
    "s_add_u32 s56, s56, 16384\n" \
    "s_addc_u32 s57, s57, 0\n" \
    "s_add_u32 m0, s62, 8192\n" \
    "s_nop 0\n" \
    "global_load_lds_dwordx4 v224, s[56:57]\n" \
    "global_load_lds_dwordx4 v225, s[56:57] offset:1024\n" \
    "s_add_u32 s56, s56, 16384\n" \
    "s_addc_u32 s57, s57, 0\n" \
    "s_add_u32 m0, s62, 10240\n" \
    "s_nop 0\n" \
    "global_load_lds_dwordx4 v224, s[56:57]\n" \
    "global_load_lds_dwordx4 v225, s[56:57] offset:1024\n" \
    "s_add_u32 s56, s56, 16384\n" \
    "s_addc_u32 s57, s57, 0\n" \
    "s_add_u32 m0, s62, 12288\n" \
    "s_nop 0\n" \
    "global_load_lds_dwordx4 v224, s[56:57]\n" \
    "global_load_lds_dwordx4 v225, s[56:57] offset:1024\n" \
    "s_add_u32 s56, s56, 16384\n" \
    "s_addc_u32 s57, s57, 0\n" \
    "s_add_u32 m0, s62, 14336\n" \
    "s_nop 0\n" \
    "global_load_lds_dwordx4 v224, s[56:57]\n" \
    "global_load_lds_dwordx4 v225, s[56:57] offset:1024\n" \
    "s_add_u32 s56, s56, 16384\n" \
    "s_addc_u32 s57, s57, 0\n" \
    "s_mov_b32 m0, s63\n"
#define G3_ASM \
    "s_mov_b32 s52, 0\n" \
    "v_add_u32 v234, 4096, v226\n" \
    "v_add_u32 v235, 8192, v226\n" \
    "v_add_u32 v236, 12288, v226\n" \
    "ds_read_b64_tr_b16 v[192:193], v230 offset:0\n" \
    "ds_read_b64_tr_b16 v[194:195], v230 offset:1024\n" \
    "ds_read_b64_tr_b16 v[196:197], v231 offset:0\n" \
    "ds_read_b64_tr_b16 v[198:199], v231 offset:1024\n" \
    "ds_read_b64_tr_b16 v[200:201], v230 offset:2048\n" \
    "ds_read_b64_tr_b16 v[202:203], v230 offset:3072\n" \
    "ds_read_b64_tr_b16 v[204:205], v231 offset:2048\n" \
    "ds_read_b64_tr_b16 v[206:207], v231 offset:3072\n" \
    "ds_read_b64_tr_b16 v[208:209], v230 offset:4096\n" \
    "ds_read_b64_tr_b16 v[210:211], v230 offset:5120\n" \
    "ds_read_b64_tr_b16 v[212:213], v231 offset:4096\n" \
    "ds_read_b64_tr_b16 v[214:215], v231 offset:5120\n" \
    "L_full_%=:\n" \
    "global_load_dwordx4 v[128:131], v226, s[58:59] offset:0 sc1\n" \
    "global_load_dwordx4 v[132:135], v226, s[58:59] offset:1024 sc1\n" \
    "global_load_dwordx4 v[136:139], v226, s[58:59] offset:2048 sc1\n" \
    "global_load_dwordx4 v[140:143], v226, s[58:59] offset:3072 sc1\n" \
    "global_load_dwordx4 v[144:147], v234, s[58:59] offset:0 sc1\n" \
    "global_load_dwordx4 v[148:151], v234, s[58:59] offset:1024 sc1\n" \
    "global_load_dwordx4 v[152:155], v234, s[58:59] offset:2048 sc1\n" \
    "global_load_dwordx4 v[156:159], v234, s[58:59] offset:3072 sc1\n" \
    "global_load_dwordx4 v[160:163], v235, s[58:59] offset:0 sc1\n" \
    "global_load_dwordx4 v[164:167], v235, s[58:59] offset:1024 sc1\n" \
    "global_load_dwordx4 v[168:171], v235, s[58:59] offset:2048 sc1\n" \
    "global_load_dwordx4 v[172:175], v235, s[58:59] offset:3072 sc1\n" \
    "global_load_dwordx4 v[176:179], v236, s[58:59] offset:0 sc1\n" \
    "global_load_dwordx4 v[180:183], v236, s[58:59] offset:1024 sc1\n" \
    "global_load_dwordx4 v[184:187], v236, s[58:59] offset:2048 sc1\n" \
    "global_load_dwordx4 v[188:191], v236, s[58:59] offset:3072 sc1\n" \
    "s_waitcnt vmcnt(0)\n" \
    "v_max3_u32 v228, v128, v129, v130\n" \
    "v_max3_u32 v229, v131, v132, v133\n" \
    "v_max3_u32 v228, v228, v134, v135\n" \
    "v_max3_u32 v229, v229, v136, v137\n" \
    "v_max3_u32 v228, v228, v138, v139\n" \
    "v_max3_u32 v229, v229, v140, v141\n" \
    "v_max3_u32 v228, v228, v142, v143\n" \
    "v_max3_u32 v229, v229, v144, v145\n" \
    "v_max3_u32 v228, v228, v146, v147\n" \
    "v_max3_u32 v229, v229, v148, v149\n" \
    "v_max3_u32 v228, v228, v150, v151\n" \
    "v_max3_u32 v229, v229, v152, v153\n" \
    "v_max3_u32 v228, v228, v154, v155\n" \
    "v_max3_u32 v229, v229, v156, v157\n" \
    "v_max3_u32 v228, v228, v158, v159\n" \
    "v_max3_u32 v229, v229, v160, v161\n" \
    "v_max3_u32 v228, v228, v162, v163\n" \
    "v_max3_u32 v229, v229, v164, v165\n" \
    "v_max3_u32 v228, v228, v166, v167\n" \
    "v_max3_u32 v229, v229, v168, v169\n" \
    "v_max3_u32 v228, v228, v170, v171\n" \
    "v_max3_u32 v229, v229, v172, v173\n" \
    "v_max3_u32 v228, v228, v174, v175\n" \
    "v_max3_u32 v229, v229, v176, v177\n" \
    "v_max3_u32 v228, v228, v178, v179\n" \
    "v_max3_u32 v229, v229, v180, v181\n" \
    "v_max3_u32 v228, v228, v182, v183\n" \
    "v_max3_u32 v229, v229, v184, v185\n" \
    "v_max3_u32 v228, v228, v186, v187\n" \
    "v_max3_u32 v229, v229, v188, v189\n" \
    "v_max3_u32 v228, v228, v190, v191\n" \
    "v_max_u32 v228, v228, v229\n" \
    "v_cmp_eq_u32 vcc, -1, v228\n" \
    "s_cbranch_vccz L_go_%=\n" \
    "s_add_u32 s52, s52, 1\n" \
    "s_cmp_lt_u32 s52, 200000\n" \
    "s_cbranch_scc1 L_full_%=\n" \
    "L_go_%=:\n" \
    "s_waitcnt lgkmcnt(8)\n" \
    "v_mfma_f32_32x32x16_bf16 v[64:79], v[192:195], v[128:131], 0\n" \
    "v_mfma_f32_32x32x16_bf16 v[80:95], v[196:199], v[128:131], 0\n" \
    "ds_read_b64_tr_b16 v[216:217], v230 offset:6144\n" \
    "ds_read_b64_tr_b16 v[218:219], v230 offset:7168\n" \
    "ds_read_b64_tr_b16 v[220:221], v231 offset:6144\n" \
    "ds_read_b64_tr_b16 v[222:223], v231 offset:7168\n" \
    "s_waitcnt lgkmcnt(8)\n" \
    "v_mfma_f32_32x32x16_bf16 v[64:79], v[200:203], v[132:135], v[64:79]\n" \
    "v_mfma_f32_32x32x16_bf16 v[80:95], v[204:207], v[132:135], v[80:95]\n" \
    "ds_read_b64_tr_b16 v[192:193], v230 offset:8192\n" \
    "ds_read_b64_tr_b16 v[194:195], v230 offset:9216\n" \
    "ds_read_b64_tr_b16 v[196:197], v231 offset:8192\n" \
    "ds_read_b64_tr_b16 v[198:199], v231 offset:9216\n" \
    "s_waitcnt lgkmcnt(8)\n" \
    "v_mfma_f32_32x32x16_bf16 v[64:79], v[208:211], v[136:139], v[64:79]\n" \
    "v_mfma_f32_32x32x16_bf16 v[80:95], v[212:215], v[136:139], v[80:95]\n" \
    "ds_read_b64_tr_b16 v[200:201], v230 offset:10240\n" \
    "ds_read_b64_tr_b16 v[202:203], v230 offset:11264\n" \
    "ds_read_b64_tr_b16 v[204:205], v231 offset:10240\n" \
    "ds_read_b64_tr_b16 v[206:207], v231 offset:11264\n" \
    "s_waitcnt lgkmcnt(8)\n" \
    "v_mfma_f32_32x32x16_bf16 v[64:79], v[216:219], v[140:143], v[64:79]\n" \
    "v_mfma_f32_32x32x16_bf16 v[80:95], v[220:223], v[140:143], v[80:95]\n" \
    "ds_read_b64_tr_b16 v[208:209], v230 offset:12288\n" \
    "ds_read_b64_tr_b16 v[210:211], v230 offset:13312\n" \
    "ds_read_b64_tr_b16 v[212:213], v231 offset:12288\n" \
    "ds_read_b64_tr_b16 v[214:215], v231 offset:13312\n" \
    "s_waitcnt lgkmcnt(8)\n" \
    "v_mfma_f32_32x32x16_bf16 v[64:79], v[192:195], v[144:147], v[64:79]\n" \
    "v_mfma_f32_32x32x16_bf16 v[80:95], v[196:199], v[144:147], v[80:95]\n" \
    "ds_read_b64_tr_b16 v[216:217], v230 offset:14336\n" \
    "ds_read_b64_tr_b16 v[218:219], v230 offset:15360\n" \
    "ds_read_b64_tr_b16 v[220:221], v231 offset:14336\n" \
    "ds_read_b64_tr_b16 v[222:223], v231 offset:15360\n" \
    "s_waitcnt lgkmcnt(8)\n" \
    "v_mfma_f32_32x32x16_bf16 v[64:79], v[200:203], v[148:151], v[64:79]\n" \
    "v_mfma_f32_32x32x16_bf16 v[80:95], v[204:207], v[148:151], v[80:95]\n" \
    "ds_read_b64_tr_b16 v[192:193], v232 offset:0\n" \
    "ds_read_b64_tr_b16 v[194:195], v232 offset:1024\n" \
    "ds_read_b64_tr_b16 v[196:197], v233 offset:0\n" \
    "ds_read_b64_tr_b16 v[198:199], v233 offset:1024\n" \
    "s_waitcnt lgkmcnt(8)\n" \
    "v_mfma_f32_32x32x16_bf16 v[64:79], v[208:211], v[152:155], v[64:79]\n" \
    "v_mfma_f32_32x32x16_bf16 v[80:95], v[212:215], v[152:155], v[80:95]\n" \
    "ds_read_b64_tr_b16 v[200:201], v232 offset:2048\n" \
    "ds_read_b64_tr_b16 v[202:203], v232 offset:3072\n" \
    "ds_read_b64_tr_b16 v[204:205], v233 offset:2048\n" \
    "ds_read_b64_tr_b16 v[206:207], v233 offset:3072\n" \
    "s_waitcnt lgkmcnt(8)\n" \
    "v_mfma_f32_32x32x16_bf16 v[64:79], v[216:219], v[156:159], v[64:79]\n" \
    "v_mfma_f32_32x32x16_bf16 v[80:95], v[220:223], v[156:159], v[80:95]\n" \
    "ds_read_b64_tr_b16 v[208:209], v232 offset:4096\n" \
    "ds_read_b64_tr_b16 v[210:211], v232 offset:5120\n" \
    "ds_read_b64_tr_b16 v[212:213], v233 offset:4096\n" \
    "ds_read_b64_tr_b16 v[214:215], v233 offset:5120\n" \
    "s_waitcnt lgkmcnt(8)\n" \
    "v_mfma_f32_32x32x16_bf16 v[64:79], v[192:195], v[160:163], v[64:79]\n" \
    "v_mfma_f32_32x32x16_bf16 v[80:95], v[196:199], v[160:163], v[80:95]\n" \
    "ds_read_b64_tr_b16 v[216:217], v232 offset:6144\n" \
    "ds_read_b64_tr_b16 v[218:219], v232 offset:7168\n" \
    "ds_read_b64_tr_b16 v[220:221], v233 offset:6144\n" \
    "ds_read_b64_tr_b16 v[222:223], v233 offset:7168\n" \
    "s_waitcnt lgkmcnt(8)\n" \
    "v_mfma_f32_32x32x16_bf16 v[64:79], v[200:203], v[164:167], v[64:79]\n" \
    "v_mfma_f32_32x32x16_bf16 v[80:95], v[204:207], v[164:167], v[80:95]\n" \
    "ds_read_b64_tr_b16 v[192:193], v232 offset:8192\n" \
    "ds_read_b64_tr_b16 v[194:195], v232 offset:9216\n" \
    "ds_read_b64_tr_b16 v[196:197], v233 offset:8192\n" \
    "ds_read_b64_tr_b16 v[198:199], v233 offset:9216\n" \
    "s_waitcnt lgkmcnt(8)\n" \
    "v_mfma_f32_32x32x16_bf16 v[64:79], v[208:211], v[168:171], v[64:79]\n" \
    "v_mfma_f32_32x32x16_bf16 v[80:95], v[212:215], v[168:171], v[80:95]\n" \
    "ds_read_b64_tr_b16 v[200:201], v232 offset:10240\n" \
    "ds_read_b64_tr_b16 v[202:203], v232 offset:11264\n" \
    "ds_read_b64_tr_b16 v[204:205], v233 offset:10240\n" \
    "ds_read_b64_tr_b16 v[206:207], v233 offset:11264\n" \
    "s_waitcnt lgkmcnt(8)\n" \
    "v_mfma_f32_32x32x16_bf16 v[64:79], v[216:219], v[172:175], v[64:79]\n" \
    "v_mfma_f32_32x32x16_bf16 v[80:95], v[220:223], v[172:175], v[80:95]\n" \
    "ds_read_b64_tr_b16 v[208:209], v232 offset:12288\n" \
    "ds_read_b64_tr_b16 v[210:211], v232 offset:13312\n" \
    "ds_read_b64_tr_b16 v[212:213], v233 offset:12288\n" \
    "ds_read_b64_tr_b16 v[214:215], v233 offset:13312\n" \
    "s_waitcnt lgkmcnt(8)\n" \
    "v_mfma_f32_32x32x16_bf16 v[64:79], v[192:195], v[176:179], v[64:79]\n" \
    "v_mfma_f32_32x32x16_bf16 v[80:95], v[196:199], v[176:179], v[80:95]\n" \
    "ds_read_b64_tr_b16 v[216:217], v232 offset:14336\n" \
    "ds_read_b64_tr_b16 v[218:219], v232 offset:15360\n" \
    "ds_read_b64_tr_b16 v[220:221], v233 offset:14336\n" \
    "ds_read_b64_tr_b16 v[222:223], v233 offset:15360\n" \
    "s_waitcnt lgkmcnt(8)\n" \
    "v_mfma_f32_32x32x16_bf16 v[64:79], v[200:203], v[180:183], v[64:79]\n" \
    "v_mfma_f32_32x32x16_bf16 v[80:95], v[204:207], v[180:183], v[80:95]\n" \
    "s_waitcnt lgkmcnt(4)\n" \
    "v_mfma_f32_32x32x16_bf16 v[64:79], v[208:211], v[184:187], v[64:79]\n" \
    "v_mfma_f32_32x32x16_bf16 v[80:95], v[212:215], v[184:187], v[80:95]\n" \
    "s_waitcnt lgkmcnt(0)\n" \
    "v_mfma_f32_32x32x16_bf16 v[64:79], v[216:219], v[188:191], v[64:79]\n" \
    "v_mfma_f32_32x32x16_bf16 v[80:95], v[220:223], v[188:191], v[80:95]\n" \
    "s_nop 15\n"
#define G3_CLOB "v128", "v129", "v130", "v131", "v132", "v133", "v134", "v135", "v136", "v137", "v138", "v139", "v140", "v141", "v142", "v143", "v144", "v145", "v146", "v147", "v148", "v149", "v150", "v151", "v152", "v153", "v154", "v155", "v156", "v157", "v158", "v159", "v160", "v161", "v162", "v163", "v164", "v165", "v166", "v167", "v168", "v169", "v170", "v171", "v172", "v173", "v174", "v175", "v176", "v177", "v178", "v179", "v180", "v181", "v182", "v183", "v184", "v185", "v186", "v187", "v188", "v189", "v190", "v191", "v192", "v193", "v194", "v195", "v196", "v197", "v198", "v199", "v200", "v201", "v202", "v203", "v204", "v205", "v206", "v207", "v208", "v209", "v210", "v211", "v212", "v213", "v214", "v215", "v216", "v217", "v218", "v219", "v220", "v221", "v222", "v223", "v228", "v229", "v234", "v235", "v236", "s52", "vcc", "memory", "scc"
#define GEMM_CLOB "v128", "v129", "v130", "v131", "v132", "v133", "v134", "v135", "v136", "v137", "v138", "v139", "v140", "v141", "v142", "v143", "v144", "v145", "v146", "v147", "v148", "v149", "v150", "v151", "v152", "v153", "v154", "v155", "v156", "v157", "v158", "v159", "v160", "v161", "v162", "v163", "v164", "v165", "v166", "v167", "v168", "v169", "v170", "v171", "v172", "v173", "v174", "v175", "s63", "vcc", "memory", "scc"

#define P1Q_ASM \
    "s_mov_b32 s63, m0\n" \
    "s_waitcnt vmcnt(1) lgkmcnt(0)\n" \
    "s_barrier\n" \
    "ds_read_b128 v[56:59], v72 offset:0\n" \
    "ds_read_b64_tr_b16 v[60:61], v80 offset:0\n" \
    "ds_read_b64_tr_b16 v[62:63], v80 offset:512\n" \
    "ds_read_b128 v[64:67], v73 offset:0\n" \
    "ds_read_b64_tr_b16 v[68:69], v80 offset:2048\n" \
    "ds_read_b64_tr_b16 v[70:71], v80 offset:2560\n" \
    "s_add_u32 m0, s62, 98304\n" \
    "s_nop 0\n" \
    "global_load_lds_dwordx4 v82, s[56:57]\n" \
    "s_waitcnt lgkmcnt(3)\n" \
    "v_mfma_f32_32x32x16_bf16 v[40:55], v[60:63], v[56:59], 0\n" \
    "ds_read_b128 v[56:59], v74 offset:0\n" \
    "ds_read_b64_tr_b16 v[60:61], v80 offset:4096\n" \
    "ds_read_b64_tr_b16 v[62:63], v80 offset:4608\n" \
    "s_add_u32 m0, s62, 99328\n" \
    "s_nop 0\n" \
    "global_load_lds_dwordx4 v83, s[56:57]\n" \
    "s_waitcnt lgkmcnt(3)\n" \
    "v_mfma_f32_32x32x16_bf16 v[40:55], v[68:71], v[64:67], v[40:55]\n" \
    "ds_read_b128 v[64:67], v75 offset:0\n" \
    "ds_read_b64_tr_b16 v[68:69], v80 offset:6144\n" \
    "ds_read_b64_tr_b16 v[70:71], v80 offset:6656\n" \
    "s_add_u32 m0, s62, 106496\n" \
    "s_nop 0\n" \
    "global_load_lds_dwordx4 v84, s[58:59] offset:0\n" \
    "s_add_u32 s56, s56, 128\n" \
    "s_addc_u32 s57, s57, 0\n" \
    "s_add_u32 s58, s58, 196608\n" \
    "s_addc_u32 s59, s59, 0\n" \
    "s_waitcnt lgkmcnt(3)\n" \
    "v_mfma_f32_32x32x16_bf16 v[40:55], v[60:63], v[56:59], v[40:55]\n" \
    "s_waitcnt lgkmcnt(0)\n" \
    "v_mfma_f32_32x32x16_bf16 v[40:55], v[68:71], v[64:67], v[40:55]\n" \
    "s_waitcnt lgkmcnt(0)\n" \
    "s_barrier\n" \
    "ds_read_b128 v[56:59], v72 offset:32768\n" \
    "ds_read_b64_tr_b16 v[60:61], v80 offset:32768\n" \
    "ds_read_b64_tr_b16 v[62:63], v80 offset:33280\n" \
    "ds_read_b128 v[64:67], v73 offset:32768\n" \
    "ds_read_b64_tr_b16 v[68:69], v80 offset:34816\n" \
    "ds_read_b64_tr_b16 v[70:71], v80 offset:35328\n" \
    "s_add_u32 m0, s62, 0\n" \
    "s_nop 0\n" \
    "global_load_lds_dwordx4 v82, s[56:57]\n" \
    "s_waitcnt lgkmcnt(3)\n" \
    "v_mfma_f32_32x32x16_bf16 v[40:55], v[60:63], v[56:59], v[40:55]\n" \
    "ds_read_b128 v[56:59], v74 offset:32768\n" \
    "ds_read_b64_tr_b16 v[60:61], v80 offset:36864\n" \
    "ds_read_b64_tr_b16 v[62:63], v80 offset:37376\n" \
    "s_add_u32 m0, s62, 1024\n" \
    "s_nop 0\n" \
    "global_load_lds_dwordx4 v83, s[56:57]\n" \
    "s_waitcnt lgkmcnt(3)\n" \
    "v_mfma_f32_32x32x16_bf16 v[40:55], v[68:71], v[64:67], v[40:55]\n" \
    "ds_read_b128 v[64:67], v75 offset:32768\n" \
    "ds_read_b64_tr_b16 v[68:69], v80 offset:38912\n" \
    "ds_read_b64_tr_b16 v[70:71], v80 offset:39424\n" \
    "s_add_u32 m0, s62, 8192\n" \
    "s_nop 0\n" \
    "global_load_lds_dwordx4 v84, s[58:59] offset:0\n" \
    "s_add_u32 s56, s56, 128\n" \
    "s_addc_u32 s57, s57, 0\n" \
    "s_add_u32 s58, s58, 196608\n" \
    "s_addc_u32 s59, s59, 0\n" \
    "s_waitcnt lgkmcnt(3)\n" \
    "v_mfma_f32_32x32x16_bf16 v[40:55], v[60:63], v[56:59], v[40:55]\n" \
    "s_waitcnt lgkmcnt(0)\n" \
    "v_mfma_f32_32x32x16_bf16 v[40:55], v[68:71], v[64:67], v[40:55]\n" \
    "s_waitcnt lgkmcnt(0)\n" \
    "s_barrier\n" \
    "ds_read_b128 v[56:59], v76 offset:0\n" \
    "ds_read_b64_tr_b16 v[60:61], v81 offset:0\n" \
    "ds_read_b64_tr_b16 v[62:63], v81 offset:512\n" \
    "ds_read_b128 v[64:67], v77 offset:0\n" \
    "ds_read_b64_tr_b16 v[68:69], v81 offset:2048\n" \
    "ds_read_b64_tr_b16 v[70:71], v81 offset:2560\n" \
    "s_add_u32 m0, s62, 32768\n" \
    "s_nop 0\n" \
    "global_load_lds_dwordx4 v82, s[56:57]\n" \
    "s_waitcnt lgkmcnt(3)\n" \
    "v_mfma_f32_32x32x16_bf16 v[40:55], v[60:63], v[56:59], v[40:55]\n" \
    "ds_read_b128 v[56:59], v78 offset:0\n" \
    "ds_read_b64_tr_b16 v[60:61], v81 offset:4096\n" \
    "ds_read_b64_tr_b16 v[62:63], v81 offset:4608\n" \
    "s_add_u32 m0, s62, 33792\n" \
    "s_nop 0\n" \
    "global_load_lds_dwordx4 v83, s[56:57]\n" \
    "s_waitcnt lgkmcnt(3)\n" \
    "v_mfma_f32_32x32x16_bf16 v[40:55], v[68:71], v[64:67], v[40:55]\n" \
    "ds_read_b128 v[64:67], v79 offset:0\n" \
    "ds_read_b64_tr_b16 v[68:69], v81 offset:6144\n" \
    "ds_read_b64_tr_b16 v[70:71], v81 offset:6656\n" \
    "s_add_u32 m0, s62, 40960\n" \
    "s_nop 0\n" \
    "global_load_lds_dwordx4 v84, s[58:59] offset:0\n" \
    "s_add_u32 s56, s56, 128\n" \
    "s_addc_u32 s57, s57, 0\n" \
    "s_add_u32 s58, s58, 196608\n" \
    "s_addc_u32 s59, s59, 0\n" \
    "s_waitcnt lgkmcnt(3)\n" \
    "v_mfma_f32_32x32x16_bf16 v[40:55], v[60:63], v[56:59], v[40:55]\n" \
    "s_waitcnt lgkmcnt(0)\n" \
    "v_mfma_f32_32x32x16_bf16 v[40:55], v[68:71], v[64:67], v[40:55]\n" \
    "s_waitcnt vmcnt(6) lgkmcnt(0)\n" \
    "s_barrier\n" \
    "ds_read_b128 v[56:59], v76 offset:32768\n" \
    "ds_read_b64_tr_b16 v[60:61], v81 offset:32768\n" \
    "ds_read_b64_tr_b16 v[62:63], v81 offset:33280\n" \
    "ds_read_b128 v[64:67], v77 offset:32768\n" \
    "ds_read_b64_tr_b16 v[68:69], v81 offset:34816\n" \
    "ds_read_b64_tr_b16 v[70:71], v81 offset:35328\n" \
    "s_add_u32 m0, s62, 65536\n" \
    "s_nop 0\n" \
    "global_load_lds_dwordx4 v82, s[56:57]\n" \
    "s_waitcnt lgkmcnt(3)\n" \
    "v_mfma_f32_32x32x16_bf16 v[40:55], v[60:63], v[56:59], v[40:55]\n" \
    "ds_read_b128 v[56:59], v78 offset:32768\n" \
    "ds_read_b64_tr_b16 v[60:61], v81 offset:36864\n" \
    "ds_read_b64_tr_b16 v[62:63], v81 offset:37376\n" \
    "s_add_u32 m0, s62, 66560\n" \
    "s_nop 0\n" \
    "global_load_lds_dwordx4 v83, s[56:57]\n" \
    "s_waitcnt lgkmcnt(3)\n" \
    "v_mfma_f32_32x32x16_bf16 v[40:55], v[68:71], v[64:67], v[40:55]\n" \
    "ds_read_b128 v[64:67], v79 offset:32768\n" \
    "ds_read_b64_tr_b16 v[68:69], v81 offset:38912\n" \
    "ds_read_b64_tr_b16 v[70:71], v81 offset:39424\n" \
    "s_add_u32 m0, s62, 73728\n" \
    "s_nop 0\n" \
    "global_load_lds_dwordx4 v84, s[58:59] offset:0\n" \
    "s_add_u32 s56, s56, 128\n" \
    "s_addc_u32 s57, s57, 0\n" \
    "s_add_u32 s58, s58, 196608\n" \
    "s_addc_u32 s59, s59, 0\n" \
    "s_waitcnt lgkmcnt(3)\n" \
    "v_mfma_f32_32x32x16_bf16 v[40:55], v[60:63], v[56:59], v[40:55]\n" \
    "s_waitcnt lgkmcnt(0)\n" \
    "v_mfma_f32_32x32x16_bf16 v[40:55], v[68:71], v[64:67], v[40:55]\n" \
    "s_waitcnt vmcnt(6) lgkmcnt(0)\n" \
    "s_barrier\n" \
    "ds_read_b128 v[56:59], v72 offset:0\n" \
    "ds_read_b64_tr_b16 v[60:61], v80 offset:0\n" \
    "ds_read_b64_tr_b16 v[62:63], v80 offset:512\n" \
    "ds_read_b128 v[64:67], v73 offset:0\n" \
    "ds_read_b64_tr_b16 v[68:69], v80 offset:2048\n" \
    "ds_read_b64_tr_b16 v[70:71], v80 offset:2560\n" \
    "s_add_u32 m0, s62, 98304\n" \
    "s_nop 0\n" \
    "global_load_lds_dwordx4 v82, s[56:57]\n" \
    "s_waitcnt lgkmcnt(3)\n" \
    "v_mfma_f32_32x32x16_bf16 v[40:55], v[60:63], v[56:59], v[40:55]\n" \
    "ds_read_b128 v[56:59], v74 offset:0\n" \
    "ds_read_b64_tr_b16 v[60:61], v80 offset:4096\n" \
    "ds_read_b64_tr_b16 v[62:63], v80 offset:4608\n" \
    "s_add_u32 m0, s62, 99328\n" \
    "s_nop 0\n" \
    "global_load_lds_dwordx4 v83, s[56:57]\n" \
    "s_waitcnt lgkmcnt(3)\n" \
    "v_mfma_f32_32x32x16_bf16 v[40:55], v[68:71], v[64:67], v[40:55]\n" \
    "ds_read_b128 v[64:67], v75 offset:0\n" \
    "ds_read_b64_tr_b16 v[68:69], v80 offset:6144\n" \
    "ds_read_b64_tr_b16 v[70:71], v80 offset:6656\n" \
    "s_add_u32 m0, s62, 106496\n" \
    "s_nop 0\n" \
    "global_load_lds_dwordx4 v84, s[58:59] offset:0\n" \
    "s_add_u32 s56, s56, 128\n" \
    "s_addc_u32 s57, s57, 0\n" \
    "s_add_u32 s58, s58, 196608\n" \
    "s_addc_u32 s59, s59, 0\n" \
    "s_waitcnt lgkmcnt(3)\n" \
    "v_mfma_f32_32x32x16_bf16 v[40:55], v[60:63], v[56:59], v[40:55]\n" \
    "s_waitcnt lgkmcnt(0)\n" \
    "v_mfma_f32_32x32x16_bf16 v[40:55], v[68:71], v[64:67], v[40:55]\n" \
    "s_waitcnt vmcnt(6) lgkmcnt(0)\n" \
    "s_barrier\n" \
    "ds_read_b128 v[56:59], v72 offset:32768\n" \
    "ds_read_b64_tr_b16 v[60:61], v80 offset:32768\n" \
    "ds_read_b64_tr_b16 v[62:63], v80 offset:33280\n" \
    "ds_read_b128 v[64:67], v73 offset:32768\n" \
    "ds_read_b64_tr_b16 v[68:69], v80 offset:34816\n" \
    "ds_read_b64_tr_b16 v[70:71], v80 offset:35328\n" \
    "s_waitcnt lgkmcnt(3)\n" \
    "v_mfma_f32_32x32x16_bf16 v[40:55], v[60:63], v[56:59], v[40:55]\n" \
    "ds_read_b128 v[56:59], v74 offset:32768\n" \
    "ds_read_b64_tr_b16 v[60:61], v80 offset:36864\n" \
    "ds_read_b64_tr_b16 v[62:63], v80 offset:37376\n" \
    "s_waitcnt lgkmcnt(3)\n" \
    "v_mfma_f32_32x32x16_bf16 v[40:55], v[68:71], v[64:67], v[40:55]\n" \
    "ds_read_b128 v[64:67], v75 offset:32768\n" \
    "ds_read_b64_tr_b16 v[68:69], v80 offset:38912\n" \
    "ds_read_b64_tr_b16 v[70:71], v80 offset:39424\n" \
    "s_waitcnt lgkmcnt(3)\n" \
    "v_mfma_f32_32x32x16_bf16 v[40:55], v[60:63], v[56:59], v[40:55]\n" \
    "s_waitcnt lgkmcnt(0)\n" \
    "v_mfma_f32_32x32x16_bf16 v[40:55], v[68:71], v[64:67], v[40:55]\n" \
    "s_waitcnt vmcnt(3) lgkmcnt(0)\n" \
    "s_barrier\n" \
    "ds_read_b128 v[56:59], v76 offset:0\n" \
    "ds_read_b64_tr_b16 v[60:61], v81 offset:0\n" \
    "ds_read_b64_tr_b16 v[62:63], v81 offset:512\n" \
    "ds_read_b128 v[64:67], v77 offset:0\n" \
    "ds_read_b64_tr_b16 v[68:69], v81 offset:2048\n" \
    "ds_read_b64_tr_b16 v[70:71], v81 offset:2560\n" \
    "s_waitcnt lgkmcnt(3)\n" \
    "v_mfma_f32_32x32x16_bf16 v[40:55], v[60:63], v[56:59], v[40:55]\n" \
    "ds_read_b128 v[56:59], v78 offset:0\n" \
    "ds_read_b64_tr_b16 v[60:61], v81 offset:4096\n" \
    "ds_read_b64_tr_b16 v[62:63], v81 offset:4608\n" \
    "s_waitcnt lgkmcnt(3)\n" \
    "v_mfma_f32_32x32x16_bf16 v[40:55], v[68:71], v[64:67], v[40:55]\n" \
    "ds_read_b128 v[64:67], v79 offset:0\n" \
    "ds_read_b64_tr_b16 v[68:69], v81 offset:6144\n" \
    "ds_read_b64_tr_b16 v[70:71], v81 offset:6656\n" \
    "s_waitcnt lgkmcnt(3)\n" \
    "v_mfma_f32_32x32x16_bf16 v[40:55], v[60:63], v[56:59], v[40:55]\n" \
    "s_waitcnt lgkmcnt(0)\n" \
    "v_mfma_f32_32x32x16_bf16 v[40:55], v[68:71], v[64:67], v[40:55]\n" \
    "s_waitcnt vmcnt(0) lgkmcnt(0)\n" \
    "s_barrier\n" \
    "ds_read_b128 v[56:59], v76 offset:32768\n" \
    "ds_read_b64_tr_b16 v[60:61], v81 offset:32768\n" \
    "ds_read_b64_tr_b16 v[62:63], v81 offset:33280\n" \
    "ds_read_b128 v[64:67], v77 offset:32768\n" \
    "ds_read_b64_tr_b16 v[68:69], v81 offset:34816\n" \
    "ds_read_b64_tr_b16 v[70:71], v81 offset:35328\n" \
    "s_waitcnt lgkmcnt(3)\n" \
    "v_mfma_f32_32x32x16_bf16 v[40:55], v[60:63], v[56:59], v[40:55]\n" \
    "ds_read_b128 v[56:59], v78 offset:32768\n" \
    "ds_read_b64_tr_b16 v[60:61], v81 offset:36864\n" \
    "ds_read_b64_tr_b16 v[62:63], v81 offset:37376\n" \
    "s_waitcnt lgkmcnt(3)\n" \
    "v_mfma_f32_32x32x16_bf16 v[40:55], v[68:71], v[64:67], v[40:55]\n" \
    "ds_read_b128 v[64:67], v79 offset:32768\n" \
    "ds_read_b64_tr_b16 v[68:69], v81 offset:38912\n" \
    "ds_read_b64_tr_b16 v[70:71], v81 offset:39424\n" \
    "s_waitcnt lgkmcnt(3)\n" \
    "v_mfma_f32_32x32x16_bf16 v[40:55], v[60:63], v[56:59], v[40:55]\n" \
    "s_waitcnt lgkmcnt(0)\n" \
    "v_mfma_f32_32x32x16_bf16 v[40:55], v[68:71], v[64:67], v[40:55]\n" \
    "s_nop 15\n" \
    "s_mov_b32 m0, s63\n"
#define P1K_ASM \
    "s_mov_b32 s63, m0\n" \
    "s_waitcnt lgkmcnt(0)\n" \
    "s_barrier\n" \
    "ds_read_b128 v[56:59], v72 offset:0\n" \
    "ds_read_b64_tr_b16 v[60:61], v80 offset:0\n" \
    "ds_read_b64_tr_b16 v[62:63], v80 offset:512\n" \
    "ds_read_b128 v[64:67], v73 offset:0\n" \
    "ds_read_b64_tr_b16 v[68:69], v80 offset:2048\n" \
    "ds_read_b64_tr_b16 v[70:71], v80 offset:2560\n" \
    "s_add_u32 m0, s62, 107520\n" \
    "s_nop 0\n" \
    "global_load_lds_dwordx4 v85, s[58:59] offset:0\n" \
    "s_waitcnt lgkmcnt(3)\n" \
    "v_mfma_f32_32x32x16_bf16 v[40:55], v[60:63], v[56:59], 0\n" \
    "ds_read_b128 v[56:59], v74 offset:0\n" \
    "ds_read_b64_tr_b16 v[60:61], v80 offset:4096\n" \
    "ds_read_b64_tr_b16 v[62:63], v80 offset:4608\n" \
    "s_add_u32 m0, s62, 113664\n" \
    "s_nop 0\n" \
    "global_load_lds_dwordx4 v84, s[58:59] offset:1024\n" \
    "s_waitcnt lgkmcnt(3)\n" \
    "v_mfma_f32_32x32x16_bf16 v[40:55], v[68:71], v[64:67], v[40:55]\n" \
    "ds_read_b128 v[64:67], v75 offset:0\n" \
    "ds_read_b64_tr_b16 v[68:69], v80 offset:6144\n" \
    "ds_read_b64_tr_b16 v[70:71], v80 offset:6656\n" \
    "s_add_u32 m0, s62, 114688\n" \
    "s_nop 0\n" \
    "global_load_lds_dwordx4 v85, s[58:59] offset:1024\n" \
    "s_add_u32 s58, s58, 196608\n" \
    "s_addc_u32 s59, s59, 0\n" \
    "s_waitcnt lgkmcnt(3)\n" \
    "v_mfma_f32_32x32x16_bf16 v[40:55], v[60:63], v[56:59], v[40:55]\n" \
    "s_waitcnt lgkmcnt(0)\n" \
    "v_mfma_f32_32x32x16_bf16 v[40:55], v[68:71], v[64:67], v[40:55]\n" \
    "s_waitcnt vmcnt(4) lgkmcnt(0)\n" \
    "s_barrier\n" \
    "ds_read_b128 v[56:59], v72 offset:32768\n" \
    "ds_read_b64_tr_b16 v[60:61], v80 offset:32768\n" \
    "ds_read_b64_tr_b16 v[62:63], v80 offset:33280\n" \
    "ds_read_b128 v[64:67], v73 offset:32768\n" \
    "ds_read_b64_tr_b16 v[68:69], v80 offset:34816\n" \
    "ds_read_b64_tr_b16 v[70:71], v80 offset:35328\n" \
    "s_add_u32 m0, s62, 9216\n" \
    "s_nop 0\n" \
    "global_load_lds_dwordx4 v85, s[58:59] offset:0\n" \
    "s_waitcnt lgkmcnt(3)\n" \
    "v_mfma_f32_32x32x16_bf16 v[40:55], v[60:63], v[56:59], v[40:55]\n" \
    "ds_read_b128 v[56:59], v74 offset:32768\n" \
    "ds_read_b64_tr_b16 v[60:61], v80 offset:36864\n" \
    "ds_read_b64_tr_b16 v[62:63], v80 offset:37376\n" \
    "s_add_u32 m0, s62, 15360\n" \
    "s_nop 0\n" \
    "global_load_lds_dwordx4 v84, s[58:59] offset:1024\n" \
    "s_waitcnt lgkmcnt(3)\n" \
    "v_mfma_f32_32x32x16_bf16 v[40:55], v[68:71], v[64:67], v[40:55]\n" \
    "ds_read_b128 v[64:67], v75 offset:32768\n" \
    "ds_read_b64_tr_b16 v[68:69], v80 offset:38912\n" \
    "ds_read_b64_tr_b16 v[70:71], v80 offset:39424\n" \
    "s_add_u32 m0, s62, 16384\n" \
    "s_nop 0\n" \
    "global_load_lds_dwordx4 v85, s[58:59] offset:1024\n" \
    "s_add_u32 s58, s58, 196608\n" \
    "s_addc_u32 s59, s59, 0\n" \
    "s_waitcnt lgkmcnt(3)\n" \
    "v_mfma_f32_32x32x16_bf16 v[40:55], v[60:63], v[56:59], v[40:55]\n" \
    "s_waitcnt lgkmcnt(0)\n" \
    "v_mfma_f32_32x32x16_bf16 v[40:55], v[68:71], v[64:67], v[40:55]\n" \
    "s_waitcnt lgkmcnt(0)\n" \
    "s_barrier\n" \
    "ds_read_b128 v[56:59], v76 offset:0\n" \
    "ds_read_b64_tr_b16 v[60:61], v81 offset:0\n" \
    "ds_read_b64_tr_b16 v[62:63], v81 offset:512\n" \
    "ds_read_b128 v[64:67], v77 offset:0\n" \
    "ds_read_b64_tr_b16 v[68:69], v81 offset:2048\n" \
    "ds_read_b64_tr_b16 v[70:71], v81 offset:2560\n" \
    "s_add_u32 m0, s62, 41984\n" \
    "s_nop 0\n" \
    "global_load_lds_dwordx4 v85, s[58:59] offset:0\n" \
    "s_waitcnt lgkmcnt(3)\n" \
    "v_mfma_f32_32x32x16_bf16 v[40:55], v[60:63], v[56:59], v[40:55]\n" \
    "ds_read_b128 v[56:59], v78 offset:0\n" \
    "ds_read_b64_tr_b16 v[60:61], v81 offset:4096\n" \
    "ds_read_b64_tr_b16 v[62:63], v81 offset:4608\n" \
    "s_add_u32 m0, s62, 48128\n" \
    "s_nop 0\n" \
    "global_load_lds_dwordx4 v84, s[58:59] offset:1024\n" \
    "s_waitcnt lgkmcnt(3)\n" \
    "v_mfma_f32_32x32x16_bf16 v[40:55], v[68:71], v[64:67], v[40:55]\n" \
    "ds_read_b128 v[64:67], v79 offset:0\n" \
    "ds_read_b64_tr_b16 v[68:69], v81 offset:6144\n" \
    "ds_read_b64_tr_b16 v[70:71], v81 offset:6656\n" \
    "s_add_u32 m0, s62, 49152\n" \
    "s_nop 0\n" \
    "global_load_lds_dwordx4 v85, s[58:59] offset:1024\n" \
    "s_add_u32 s58, s58, 196608\n" \
    "s_addc_u32 s59, s59, 0\n" \
    "s_waitcnt lgkmcnt(3)\n" \
    "v_mfma_f32_32x32x16_bf16 v[40:55], v[60:63], v[56:59], v[40:55]\n" \
    "s_waitcnt lgkmcnt(0)\n" \
    "v_mfma_f32_32x32x16_bf16 v[40:55], v[68:71], v[64:67], v[40:55]\n" \
    "s_waitcnt vmcnt(6) lgkmcnt(0)\n" \
    "s_barrier\n" \
    "ds_read_b128 v[56:59], v76 offset:32768\n" \
    "ds_read_b64_tr_b16 v[60:61], v81 offset:32768\n" \
    "ds_read_b64_tr_b16 v[62:63], v81 offset:33280\n" \
    "ds_read_b128 v[64:67], v77 offset:32768\n" \
    "ds_read_b64_tr_b16 v[68:69], v81 offset:34816\n" \
    "ds_read_b64_tr_b16 v[70:71], v81 offset:35328\n" \
    "s_add_u32 m0, s62, 74752\n" \
    "s_nop 0\n" \
    "global_load_lds_dwordx4 v85, s[58:59] offset:0\n" \
    "s_waitcnt lgkmcnt(3)\n" \
    "v_mfma_f32_32x32x16_bf16 v[40:55], v[60:63], v[56:59], v[40:55]\n" \
    "ds_read_b128 v[56:59], v78 offset:32768\n" \
    "ds_read_b64_tr_b16 v[60:61], v81 offset:36864\n" \
    "ds_read_b64_tr_b16 v[62:63], v81 offset:37376\n" \
    "s_add_u32 m0, s62, 80896\n" \
    "s_nop 0\n" \
    "global_load_lds_dwordx4 v84, s[58:59] offset:1024\n" \
    "s_waitcnt lgkmcnt(3)\n" \
    "v_mfma_f32_32x32x16_bf16 v[40:55], v[68:71], v[64:67], v[40:55]\n" \
    "ds_read_b128 v[64:67], v79 offset:32768\n" \
    "ds_read_b64_tr_b16 v[68:69], v81 offset:38912\n" \
    "ds_read_b64_tr_b16 v[70:71], v81 offset:39424\n" \
    "s_add_u32 m0, s62, 81920\n" \
    "s_nop 0\n" \
    "global_load_lds_dwordx4 v85, s[58:59] offset:1024\n" \
    "s_add_u32 s58, s58, 196608\n" \
    "s_addc_u32 s59, s59, 0\n" \
    "s_waitcnt lgkmcnt(3)\n" \
    "v_mfma_f32_32x32x16_bf16 v[40:55], v[60:63], v[56:59], v[40:55]\n" \
    "s_waitcnt lgkmcnt(0)\n" \
    "v_mfma_f32_32x32x16_bf16 v[40:55], v[68:71], v[64:67], v[40:55]\n" \
    "s_waitcnt vmcnt(6) lgkmcnt(0)\n" \
    "s_barrier\n" \
    "ds_read_b128 v[56:59], v72 offset:0\n" \
    "ds_read_b64_tr_b16 v[60:61], v80 offset:0\n" \
    "ds_read_b64_tr_b16 v[62:63], v80 offset:512\n" \
    "ds_read_b128 v[64:67], v73 offset:0\n" \
    "ds_read_b64_tr_b16 v[68:69], v80 offset:2048\n" \
    "ds_read_b64_tr_b16 v[70:71], v80 offset:2560\n" \
    "s_add_u32 m0, s62, 107520\n" \
    "s_nop 0\n" \
    "global_load_lds_dwordx4 v85, s[58:59] offset:0\n" \
    "s_waitcnt lgkmcnt(3)\n" \
    "v_mfma_f32_32x32x16_bf16 v[40:55], v[60:63], v[56:59], v[40:55]\n" \
    "ds_read_b128 v[56:59], v74 offset:0\n" \
    "ds_read_b64_tr_b16 v[60:61], v80 offset:4096\n" \
    "ds_read_b64_tr_b16 v[62:63], v80 offset:4608\n" \
    "s_add_u32 m0, s62, 113664\n" \
    "s_nop 0\n" \
    "global_load_lds_dwordx4 v84, s[58:59] offset:1024\n" \
    "s_waitcnt lgkmcnt(3)\n" \
    "v_mfma_f32_32x32x16_bf16 v[40:55], v[68:71], v[64:67], v[40:55]\n" \
    "ds_read_b128 v[64:67], v75 offset:0\n" \
    "ds_read_b64_tr_b16 v[68:69], v80 offset:6144\n" \
    "ds_read_b64_tr_b16 v[70:71], v80 offset:6656\n" \
    "s_add_u32 m0, s62, 114688\n" \
    "s_nop 0\n" \
    "global_load_lds_dwordx4 v85, s[58:59] offset:1024\n" \
    "s_add_u32 s58, s58, 196608\n" \
    "s_addc_u32 s59, s59, 0\n" \
    "s_waitcnt lgkmcnt(3)\n" \
    "v_mfma_f32_32x32x16_bf16 v[40:55], v[60:63], v[56:59], v[40:55]\n" \
    "s_waitcnt lgkmcnt(0)\n" \
    "v_mfma_f32_32x32x16_bf16 v[40:55], v[68:71], v[64:67], v[40:55]\n" \
    "s_waitcnt vmcnt(6) lgkmcnt(0)\n" \
    "s_barrier\n" \
    "ds_read_b128 v[56:59], v72 offset:32768\n" \
    "ds_read_b64_tr_b16 v[60:61], v80 offset:32768\n" \
    "ds_read_b64_tr_b16 v[62:63], v80 offset:33280\n" \
    "ds_read_b128 v[64:67], v73 offset:32768\n" \
    "ds_read_b64_tr_b16 v[68:69], v80 offset:34816\n" \
    "ds_read_b64_tr_b16 v[70:71], v80 offset:35328\n" \
    "s_waitcnt lgkmcnt(3)\n" \
    "v_mfma_f32_32x32x16_bf16 v[40:55], v[60:63], v[56:59], v[40:55]\n" \
    "ds_read_b128 v[56:59], v74 offset:32768\n" \
    "ds_read_b64_tr_b16 v[60:61], v80 offset:36864\n" \
    "ds_read_b64_tr_b16 v[62:63], v80 offset:37376\n" \
    "s_waitcnt lgkmcnt(3)\n" \
    "v_mfma_f32_32x32x16_bf16 v[40:55], v[68:71], v[64:67], v[40:55]\n" \
    "ds_read_b128 v[64:67], v75 offset:32768\n" \
    "ds_read_b64_tr_b16 v[68:69], v80 offset:38912\n" \
    "ds_read_b64_tr_b16 v[70:71], v80 offset:39424\n" \
    "s_waitcnt lgkmcnt(3)\n" \
    "v_mfma_f32_32x32x16_bf16 v[40:55], v[60:63], v[56:59], v[40:55]\n" \
    "s_waitcnt lgkmcnt(0)\n" \
    "v_mfma_f32_32x32x16_bf16 v[40:55], v[68:71], v[64:67], v[40:55]\n" \
    "s_waitcnt vmcnt(3) lgkmcnt(0)\n" \
    "s_barrier\n" \
    "ds_read_b128 v[56:59], v76 offset:0\n" \
    "ds_read_b64_tr_b16 v[60:61], v81 offset:0\n" \
    "ds_read_b64_tr_b16 v[62:63], v81 offset:512\n" \
    "ds_read_b128 v[64:67], v77 offset:0\n" \
    "ds_read_b64_tr_b16 v[68:69], v81 offset:2048\n" \
    "ds_read_b64_tr_b16 v[70:71], v81 offset:2560\n" \
    "s_waitcnt lgkmcnt(3)\n" \
    "v_mfma_f32_32x32x16_bf16 v[40:55], v[60:63], v[56:59], v[40:55]\n" \
    "ds_read_b128 v[56:59], v78 offset:0\n" \
    "ds_read_b64_tr_b16 v[60:61], v81 offset:4096\n" \
    "ds_read_b64_tr_b16 v[62:63], v81 offset:4608\n" \
    "s_waitcnt lgkmcnt(3)\n" \
    "v_mfma_f32_32x32x16_bf16 v[40:55], v[68:71], v[64:67], v[40:55]\n" \
    "ds_read_b128 v[64:67], v79 offset:0\n" \
    "ds_read_b64_tr_b16 v[68:69], v81 offset:6144\n" \
    "ds_read_b64_tr_b16 v[70:71], v81 offset:6656\n" \
    "s_waitcnt lgkmcnt(3)\n" \
    "v_mfma_f32_32x32x16_bf16 v[40:55], v[60:63], v[56:59], v[40:55]\n" \
    "s_waitcnt lgkmcnt(0)\n" \
    "v_mfma_f32_32x32x16_bf16 v[40:55], v[68:71], v[64:67], v[40:55]\n" \
    "s_waitcnt vmcnt(0) lgkmcnt(0)\n" \
    "s_barrier\n" \
    "ds_read_b128 v[56:59], v76 offset:32768\n" \
    "ds_read_b64_tr_b16 v[60:61], v81 offset:32768\n" \
    "ds_read_b64_tr_b16 v[62:63], v81 offset:33280\n" \
    "ds_read_b128 v[64:67], v77 offset:32768\n" \
    "ds_read_b64_tr_b16 v[68:69], v81 offset:34816\n" \
    "ds_read_b64_tr_b16 v[70:71], v81 offset:35328\n" \
    "s_waitcnt lgkmcnt(3)\n" \
    "v_mfma_f32_32x32x16_bf16 v[40:55], v[60:63], v[56:59], v[40:55]\n" \
    "ds_read_b128 v[56:59], v78 offset:32768\n" \
    "ds_read_b64_tr_b16 v[60:61], v81 offset:36864\n" \
    "ds_read_b64_tr_b16 v[62:63], v81 offset:37376\n" \
    "s_waitcnt lgkmcnt(3)\n" \
    "v_mfma_f32_32x32x16_bf16 v[40:55], v[68:71], v[64:67], v[40:55]\n" \
    "ds_read_b128 v[64:67], v79 offset:32768\n" \
    "ds_read_b64_tr_b16 v[68:69], v81 offset:38912\n" \
    "ds_read_b64_tr_b16 v[70:71], v81 offset:39424\n" \
    "s_waitcnt lgkmcnt(3)\n" \
    "v_mfma_f32_32x32x16_bf16 v[40:55], v[60:63], v[56:59], v[40:55]\n" \
    "s_waitcnt lgkmcnt(0)\n" \
    "v_mfma_f32_32x32x16_bf16 v[40:55], v[68:71], v[64:67], v[40:55]\n" \
    "s_nop 15\n" \
    "s_mov_b32 m0, s63\n"
#define P1V_ASM \
    "s_mov_b32 s63, m0\n" \
    "s_waitcnt lgkmcnt(0)\n" \
    "s_barrier\n" \
    "ds_read_b128 v[56:59], v72 offset:0\n" \
    "ds_read_b64_tr_b16 v[60:61], v80 offset:0\n" \
    "ds_read_b64_tr_b16 v[62:63], v80 offset:512\n" \
    "ds_read_b128 v[64:67], v73 offset:0\n" \
    "ds_read_b64_tr_b16 v[68:69], v80 offset:2048\n" \
    "ds_read_b64_tr_b16 v[70:71], v80 offset:2560\n" \
    "s_add_u32 m0, s62, 65536\n" \
    "s_nop 0\n" \
    "global_load_lds_dwordx4 v82, s[64:65]\n" \
    "s_add_u32 m0, s62, 66560\n" \
    "s_nop 0\n" \
    "global_load_lds_dwordx4 v83, s[64:65]\n" \
    "s_add_u32 m0, s62, 73728\n" \
    "s_nop 0\n" \
    "global_load_lds_dwordx4 v84, s[66:67] offset:0\n" \
    "s_add_u32 m0, s62, 74752\n" \
    "s_nop 0\n" \
    "global_load_lds_dwordx4 v85, s[66:67] offset:0\n" \
    "s_add_u32 m0, s62, 80896\n" \
    "s_nop 0\n" \
    "global_load_lds_dwordx4 v84, s[66:67] offset:1024\n" \
    "s_add_u32 m0, s62, 81920\n" \
    "s_nop 0\n" \
    "global_load_lds_dwordx4 v85, s[66:67] offset:1024\n" \
    "s_add_u32 m0, s62, 88064\n" \
    "s_nop 0\n" \
    "global_load_lds_dwordx4 v84, s[66:67] offset:2048\n" \
    "s_add_u32 m0, s62, 89088\n" \
    "s_nop 0\n" \
    "global_load_lds_dwordx4 v85, s[66:67] offset:2048\n" \
    "s_add_u32 m0, s62, 120832\n" \
    "s_nop 0\n" \
    "global_load_lds_dwordx4 v84, s[58:59] offset:2048\n" \
    "s_waitcnt lgkmcnt(3)\n" \
    "v_mfma_f32_32x32x16_bf16 v[40:55], v[56:59], v[60:63], 0\n" \
    "ds_read_b128 v[56:59], v74 offset:0\n" \
    "ds_read_b64_tr_b16 v[60:61], v80 offset:4096\n" \
    "ds_read_b64_tr_b16 v[62:63], v80 offset:4608\n" \
    "s_add_u32 m0, s62, 121856\n" \
    "s_nop 0\n" \
    "global_load_lds_dwordx4 v85, s[58:59] offset:2048\n" \
    "s_add_u32 s58, s58, 196608\n" \
    "s_addc_u32 s59, s59, 0\n" \
    "s_waitcnt lgkmcnt(3)\n" \
    "v_mfma_f32_32x32x16_bf16 v[40:55], v[64:67], v[68:71], v[40:55]\n" \
    "ds_read_b128 v[64:67], v75 offset:0\n" \
    "ds_read_b64_tr_b16 v[68:69], v80 offset:6144\n" \
    "ds_read_b64_tr_b16 v[70:71], v80 offset:6656\n" \
    "s_waitcnt lgkmcnt(3)\n" \
    "v_mfma_f32_32x32x16_bf16 v[40:55], v[56:59], v[60:63], v[40:55]\n" \
    "s_waitcnt lgkmcnt(0)\n" \
    "v_mfma_f32_32x32x16_bf16 v[40:55], v[64:67], v[68:71], v[40:55]\n" \
    "s_waitcnt lgkmcnt(0)\n" \
    "s_barrier\n" \
    "ds_read_b128 v[56:59], v72 offset:32768\n" \
    "ds_read_b64_tr_b16 v[60:61], v80 offset:32768\n" \
    "ds_read_b64_tr_b16 v[62:63], v80 offset:33280\n" \
    "ds_read_b128 v[64:67], v73 offset:32768\n" \
    "ds_read_b64_tr_b16 v[68:69], v80 offset:34816\n" \
    "ds_read_b64_tr_b16 v[70:71], v80 offset:35328\n" \
    "s_add_u32 m0, s62, 22528\n" \
    "s_nop 0\n" \
    "global_load_lds_dwordx4 v84, s[58:59] offset:2048\n" \
    "s_waitcnt lgkmcnt(3)\n" \
    "v_mfma_f32_32x32x16_bf16 v[40:55], v[56:59], v[60:63], v[40:55]\n" \
    "ds_read_b128 v[56:59], v74 offset:32768\n" \
    "ds_read_b64_tr_b16 v[60:61], v80 offset:36864\n" \
    "ds_read_b64_tr_b16 v[62:63], v80 offset:37376\n" \
    "s_add_u32 m0, s62, 23552\n" \
    "s_nop 0\n" \
    "global_load_lds_dwordx4 v85, s[58:59] offset:2048\n" \
    "s_add_u32 s58, s58, 196608\n" \
    "s_addc_u32 s59, s59, 0\n" \
    "s_waitcnt lgkmcnt(3)\n" \
    "v_mfma_f32_32x32x16_bf16 v[40:55], v[64:67], v[68:71], v[40:55]\n" \
    "ds_read_b128 v[64:67], v75 offset:32768\n" \
    "ds_read_b64_tr_b16 v[68:69], v80 offset:38912\n" \
    "ds_read_b64_tr_b16 v[70:71], v80 offset:39424\n" \
    "s_waitcnt lgkmcnt(3)\n" \
    "v_mfma_f32_32x32x16_bf16 v[40:55], v[56:59], v[60:63], v[40:55]\n" \
    "s_waitcnt lgkmcnt(0)\n" \
    "v_mfma_f32_32x32x16_bf16 v[40:55], v[64:67], v[68:71], v[40:55]\n" \
    "s_waitcnt vmcnt(4) lgkmcnt(0)\n" \
    "s_barrier\n" \
    "ds_read_b128 v[56:59], v76 offset:0\n" \
    "ds_read_b64_tr_b16 v[60:61], v81 offset:0\n" \
    "ds_read_b64_tr_b16 v[62:63], v81 offset:512\n" \
    "ds_read_b128 v[64:67], v77 offset:0\n" \
    "ds_read_b64_tr_b16 v[68:69], v81 offset:2048\n" \
    "ds_read_b64_tr_b16 v[70:71], v81 offset:2560\n" \
    "s_add_u32 m0, s62, 55296\n" \
    "s_nop 0\n" \
    "global_load_lds_dwordx4 v84, s[58:59] offset:2048\n" \
    "s_waitcnt lgkmcnt(3)\n" \
    "v_mfma_f32_32x32x16_bf16 v[40:55], v[56:59], v[60:63], v[40:55]\n" \
    "ds_read_b128 v[56:59], v78 offset:0\n" \
    "ds_read_b64_tr_b16 v[60:61], v81 offset:4096\n" \
    "ds_read_b64_tr_b16 v[62:63], v81 offset:4608\n" \
    "s_add_u32 m0, s62, 56320\n" \
    "s_nop 0\n" \
    "global_load_lds_dwordx4 v85, s[58:59] offset:2048\n" \
    "s_add_u32 s58, s58, 196608\n" \
    "s_addc_u32 s59, s59, 0\n" \
    "s_waitcnt lgkmcnt(3)\n" \
    "v_mfma_f32_32x32x16_bf16 v[40:55], v[64:67], v[68:71], v[40:55]\n" \
    "ds_read_b128 v[64:67], v79 offset:0\n" \
    "ds_read_b64_tr_b16 v[68:69], v81 offset:6144\n" \
    "ds_read_b64_tr_b16 v[70:71], v81 offset:6656\n" \
    "s_waitcnt lgkmcnt(3)\n" \
    "v_mfma_f32_32x32x16_bf16 v[40:55], v[56:59], v[60:63], v[40:55]\n" \
    "s_waitcnt lgkmcnt(0)\n" \
    "v_mfma_f32_32x32x16_bf16 v[40:55], v[64:67], v[68:71], v[40:55]\n" \
    "s_waitcnt vmcnt(4) lgkmcnt(0)\n" \
    "s_barrier\n" \
    "ds_read_b128 v[56:59], v76 offset:32768\n" \
    "ds_read_b64_tr_b16 v[60:61], v81 offset:32768\n" \
    "ds_read_b64_tr_b16 v[62:63], v81 offset:33280\n" \
    "ds_read_b128 v[64:67], v77 offset:32768\n" \
    "ds_read_b64_tr_b16 v[68:69], v81 offset:34816\n" \
    "ds_read_b64_tr_b16 v[70:71], v81 offset:35328\n" \
    "s_add_u32 m0, s62, 88064\n" \
    "s_nop 0\n" \
    "global_load_lds_dwordx4 v84, s[58:59] offset:2048\n" \
    "s_waitcnt lgkmcnt(3)\n" \
    "v_mfma_f32_32x32x16_bf16 v[40:55], v[56:59], v[60:63], v[40:55]\n" \
    "ds_read_b128 v[56:59], v78 offset:32768\n" \
    "ds_read_b64_tr_b16 v[60:61], v81 offset:36864\n" \
    "ds_read_b64_tr_b16 v[62:63], v81 offset:37376\n" \
    "s_add_u32 m0, s62, 89088\n" \
    "s_nop 0\n" \
    "global_load_lds_dwordx4 v85, s[58:59] offset:2048\n" \
    "s_add_u32 s58, s58, 196608\n" \
    "s_addc_u32 s59, s59, 0\n" \
    "s_waitcnt lgkmcnt(3)\n" \
    "v_mfma_f32_32x32x16_bf16 v[40:55], v[64:67], v[68:71], v[40:55]\n" \
    "ds_read_b128 v[64:67], v79 offset:32768\n" \
    "ds_read_b64_tr_b16 v[68:69], v81 offset:38912\n" \
    "ds_read_b64_tr_b16 v[70:71], v81 offset:39424\n" \
    "s_waitcnt lgkmcnt(3)\n" \
    "v_mfma_f32_32x32x16_bf16 v[40:55], v[56:59], v[60:63], v[40:55]\n" \
    "s_waitcnt lgkmcnt(0)\n" \
    "v_mfma_f32_32x32x16_bf16 v[40:55], v[64:67], v[68:71], v[40:55]\n" \
    "s_waitcnt vmcnt(4) lgkmcnt(0)\n" \
    "s_barrier\n" \
    "ds_read_b128 v[56:59], v72 offset:0\n" \
    "ds_read_b64_tr_b16 v[60:61], v80 offset:0\n" \
    "ds_read_b64_tr_b16 v[62:63], v80 offset:512\n" \
    "ds_read_b128 v[64:67], v73 offset:0\n" \
    "ds_read_b64_tr_b16 v[68:69], v80 offset:2048\n" \
    "ds_read_b64_tr_b16 v[70:71], v80 offset:2560\n" \
    "s_add_u32 m0, s62, 120832\n" \
    "s_nop 0\n" \
    "global_load_lds_dwordx4 v84, s[58:59] offset:2048\n" \
    "s_waitcnt lgkmcnt(3)\n" \
    "v_mfma_f32_32x32x16_bf16 v[40:55], v[56:59], v[60:63], v[40:55]\n" \
    "ds_read_b128 v[56:59], v74 offset:0\n" \
    "ds_read_b64_tr_b16 v[60:61], v80 offset:4096\n" \
    "ds_read_b64_tr_b16 v[62:63], v80 offset:4608\n" \
    "s_add_u32 m0, s62, 121856\n" \
    "s_nop 0\n" \
    "global_load_lds_dwordx4 v85, s[58:59] offset:2048\n" \
    "s_add_u32 s58, s58, 196608\n" \
    "s_addc_u32 s59, s59, 0\n" \
    "s_waitcnt lgkmcnt(3)\n" \
    "v_mfma_f32_32x32x16_bf16 v[40:55], v[64:67], v[68:71], v[40:55]\n" \
    "ds_read_b128 v[64:67], v75 offset:0\n" \
    "ds_read_b64_tr_b16 v[68:69], v80 offset:6144\n" \
    "ds_read_b64_tr_b16 v[70:71], v80 offset:6656\n" \
    "s_waitcnt lgkmcnt(3)\n" \
    "v_mfma_f32_32x32x16_bf16 v[40:55], v[56:59], v[60:63], v[40:55]\n" \
    "s_waitcnt lgkmcnt(0)\n" \
    "v_mfma_f32_32x32x16_bf16 v[40:55], v[64:67], v[68:71], v[40:55]\n" \
    "s_waitcnt vmcnt(4) lgkmcnt(0)\n" \
    "s_barrier\n" \
    "ds_read_b128 v[56:59], v72 offset:32768\n" \
    "ds_read_b64_tr_b16 v[60:61], v80 offset:32768\n" \
    "ds_read_b64_tr_b16 v[62:63], v80 offset:33280\n" \
    "ds_read_b128 v[64:67], v73 offset:32768\n" \
    "ds_read_b64_tr_b16 v[68:69], v80 offset:34816\n" \
    "ds_read_b64_tr_b16 v[70:71], v80 offset:35328\n" \
    "s_waitcnt lgkmcnt(3)\n" \
    "v_mfma_f32_32x32x16_bf16 v[40:55], v[56:59], v[60:63], v[40:55]\n" \
    "ds_read_b128 v[56:59], v74 offset:32768\n" \
    "ds_read_b64_tr_b16 v[60:61], v80 offset:36864\n" \
    "ds_read_b64_tr_b16 v[62:63], v80 offset:37376\n" \
    "s_waitcnt lgkmcnt(3)\n" \
    "v_mfma_f32_32x32x16_bf16 v[40:55], v[64:67], v[68:71], v[40:55]\n" \
    "ds_read_b128 v[64:67], v75 offset:32768\n" \
    "ds_read_b64_tr_b16 v[68:69], v80 offset:38912\n" \
    "ds_read_b64_tr_b16 v[70:71], v80 offset:39424\n" \
    "s_waitcnt lgkmcnt(3)\n" \
    "v_mfma_f32_32x32x16_bf16 v[40:55], v[56:59], v[60:63], v[40:55]\n" \
    "s_waitcnt lgkmcnt(0)\n" \
    "v_mfma_f32_32x32x16_bf16 v[40:55], v[64:67], v[68:71], v[40:55]\n" \
    "s_waitcnt vmcnt(2) lgkmcnt(0)\n" \
    "s_barrier\n" \
    "ds_read_b128 v[56:59], v76 offset:0\n" \
    "ds_read_b64_tr_b16 v[60:61], v81 offset:0\n" \
    "ds_read_b64_tr_b16 v[62:63], v81 offset:512\n" \
    "ds_read_b128 v[64:67], v77 offset:0\n" \
    "ds_read_b64_tr_b16 v[68:69], v81 offset:2048\n" \
    "ds_read_b64_tr_b16 v[70:71], v81 offset:2560\n" \
    "s_waitcnt lgkmcnt(3)\n" \
    "v_mfma_f32_32x32x16_bf16 v[40:55], v[56:59], v[60:63], v[40:55]\n" \
    "ds_read_b128 v[56:59], v78 offset:0\n" \
    "ds_read_b64_tr_b16 v[60:61], v81 offset:4096\n" \
    "ds_read_b64_tr_b16 v[62:63], v81 offset:4608\n" \
    "s_waitcnt lgkmcnt(3)\n" \
    "v_mfma_f32_32x32x16_bf16 v[40:55], v[64:67], v[68:71], v[40:55]\n" \
    "ds_read_b128 v[64:67], v79 offset:0\n" \
    "ds_read_b64_tr_b16 v[68:69], v81 offset:6144\n" \
    "ds_read_b64_tr_b16 v[70:71], v81 offset:6656\n" \
    "s_waitcnt lgkmcnt(3)\n" \
    "v_mfma_f32_32x32x16_bf16 v[40:55], v[56:59], v[60:63], v[40:55]\n" \
    "s_waitcnt lgkmcnt(0)\n" \
    "v_mfma_f32_32x32x16_bf16 v[40:55], v[64:67], v[68:71], v[40:55]\n" \
    "s_waitcnt vmcnt(0) lgkmcnt(0)\n" \
    "s_barrier\n" \
    "ds_read_b128 v[56:59], v76 offset:32768\n" \
    "ds_read_b64_tr_b16 v[60:61], v81 offset:32768\n" \
    "ds_read_b64_tr_b16 v[62:63], v81 offset:33280\n" \
    "ds_read_b128 v[64:67], v77 offset:32768\n" \
    "ds_read_b64_tr_b16 v[68:69], v81 offset:34816\n" \
    "ds_read_b64_tr_b16 v[70:71], v81 offset:35328\n" \
    "s_waitcnt lgkmcnt(3)\n" \
    "v_mfma_f32_32x32x16_bf16 v[40:55], v[56:59], v[60:63], v[40:55]\n" \
    "ds_read_b128 v[56:59], v78 offset:32768\n" \
    "ds_read_b64_tr_b16 v[60:61], v81 offset:36864\n" \
    "ds_read_b64_tr_b16 v[62:63], v81 offset:37376\n" \
    "s_waitcnt lgkmcnt(3)\n" \
    "v_mfma_f32_32x32x16_bf16 v[40:55], v[64:67], v[68:71], v[40:55]\n" \
    "ds_read_b128 v[64:67], v79 offset:32768\n" \
    "ds_read_b64_tr_b16 v[68:69], v81 offset:38912\n" \
    "ds_read_b64_tr_b16 v[70:71], v81 offset:39424\n" \
    "s_waitcnt lgkmcnt(3)\n" \
    "v_mfma_f32_32x32x16_bf16 v[40:55], v[56:59], v[60:63], v[40:55]\n" \
    "s_waitcnt lgkmcnt(0)\n" \
    "v_mfma_f32_32x32x16_bf16 v[40:55], v[64:67], v[68:71], v[40:55]\n" \
    "s_nop 15\n" \
    "s_mov_b32 m0, s63\n"
#define P1_CLOB "v56", "v57", "v58", "v59", "v60", "v61", "v62", "v63", "v64", "v65", "v66", "v67", "v68", "v69", "v70", "v71", "s63", "vcc", "memory", "scc"

constexpr int P2_V = 65536, P2_RING = 131072, P2_RS = 8192, P2_CNT = 0, P2_SCR = 512, P2_LDS = 163840;
constexpr unsigned SPIN_MAX = 1u << 16;
typedef __attribute__((address_space(3))) unsigned lds_u32;
typedef __attribute__((address_space(3))) f32x4 lds_f32x4;

template <int CTRL, int RM> DI float dpp_add(float v) {
  return v + __builtin_bit_cast(float, __builtin_amdgcn_update_dpp(0, __builtin_bit_cast(int, v), CTRL, RM, 0xf, true));
}
DI float wave_total(float v) {
  v = dpp_add<0x111, 0xf>(v); v = dpp_add<0x112, 0xf>(v); v = dpp_add<0x114, 0xf>(v); v = dpp_add<0x118, 0xf>(v);
  v = dpp_add<0x142, 0xa>(v); v = dpp_add<0x143, 0xc>(v);
  return __builtin_bit_cast(float, __builtin_amdgcn_readlane(__builtin_bit_cast(int, v), 63));
}
DI void lds_arrive(char* lds, int idx) {
  unsigned a = (unsigned)(uintptr_t)lds + P2_CNT + 4 * idx, one = 1;
  asm volatile("s_waitcnt lgkmcnt(0)\n\ts_mov_b64 exec, 1\n\tds_add_u32 %0, %1\n\ts_mov_b64 exec, -1" ::"v"(a), "v"(one) : "memory");
}
DI void lds_wait(char* lds, int idx, unsigned target) {
  volatile lds_u32* p = (volatile lds_u32*)((lds_ptr)lds + P2_CNT) + idx;
  while (*p < target) __builtin_amdgcn_s_sleep(1);
  asm volatile("" ::: "memory");
}
DI void lds_spin(char* lds, int idx, unsigned target) {
  volatile lds_u32* p = (volatile lds_u32*)((lds_ptr)lds + P2_CNT) + idx;
  while (*p < target) {}
  asm volatile("" ::: "memory");
}
DI void poll_words(const unsigned* p, bool active, unsigned tag) {
  for (unsigned spins = 0; spins < SPIN_MAX; ++spins) {
    const unsigned v = ld4_sc1(p);
    if (__all(!active || v == tag)) break;
    __builtin_amdgcn_s_sleep(2);
  }
}
DI void touch(const char* p, unsigned lds0) {
  unsigned sv;
  asm volatile("s_mov_b32 %0, m0\n\ts_mov_b32 m0, %2\n\ts_nop 0\n\tglobal_load_lds_dword %1, off\n\ts_mov_b32 m0, %0" : "=&s"(sv) : "v"(p), "s"(lds0 + P2_SCR) : "memory");
}
DI int lane_id() { int l; asm volatile("v_mbcnt_lo_u32_b32 %0, -1, 0\n\tv_mbcnt_hi_u32_b32 %0, -1, %0" : "=v"(l)); return l; }

DI float half_max(float m) {
  auto rr = __builtin_amdgcn_permlane32_swap(__float_as_uint(m), __float_as_uint(m), false, false);
  return __builtin_fmaxf(__uint_as_float(rr[0]), __uint_as_float(rr[1]));
}
DI float row_sum(float l) {
  auto rr = __builtin_amdgcn_permlane32_swap(__float_as_uint(l), __float_as_uint(l), false, false);
  return __uint_as_float(rr[0]);
}
DI f32x16 attn_exact(const char* kg, const char* vg, bf16x8 q, int lane) {
  const int r = lane & 31, h = lane >> 5;
  f32x16 o = {};
  float m = -INFINITY;
  const f32x16 zero = {};
  for (int t = 0; t < 64; ++t) {
    const bf16x8 kf = __builtin_bit_cast(bf16x8, ld16_sc1(kg + t * 1024 + lane * 16));
    f32x16 s = MFMA(kf, q, zero);
    float tm = s[0];
#pragma unroll
    for (int i = 1; i < 16; ++i) tm = __builtin_fmaxf(tm, s[i]);
    const float mn = __builtin_fmaxf(m, half_max(tm));
    const float al = __builtin_amdgcn_exp2f(m - mn);
    m = mn;
#pragma unroll
    for (int i = 0; i < 16; ++i) { o[i] *= al; s[i] = __builtin_amdgcn_exp2f(s[i] - mn); }
#pragma unroll
    for (int sp = 0; sp < 2; ++sp) {
      u32x4 vf = ld16_sc1(vg + t * 1024 + sp * 512 + h * 256 + (r & 15) * 16);
      if (r >= 16) vf = (u32x4){0x3f803f80u, 0x3f803f80u, 0x3f803f80u, 0x3f803f80u};
      o = MFMA(__builtin_bit_cast(bf16x8, vf), __builtin_bit_cast(bf16x8, pack8(s, 8 * sp, 1.f)), o);
    }
  }
  return o;
}
DI bool range_ok(const f32x16& o) {
  bool ok = o[8] >= 0x1p-60f && o[8] <= 0x1p60f;
#pragma unroll
  for (int i = 0; i < 8; ++i) ok = ok && (__builtin_fabsf(o[i]) <= 0x1p126f);
  return ok;
}


#define CODE_ADDR(NAME, SYM) DI const char* NAME() { const char* p; \
  asm volatile("s_getpc_b64 s[40:41]\n\ts_add_u32 s40, s40, " SYM "@rel32@lo+4\n\ts_addc_u32 s41, s41, " SYM "@rel32@hi+12" : "={s[40:41]}"(p)::"scc"); return p; }
CODE_ADDR(code_qkv, "k_qkv") CODE_ADDR(code_mid, "k_mid") CODE_ADDR(code_end, "k_end")
#undef CODE_ADDR
DI void touch_to(const char* p, unsigned lds_dst) {
  unsigned sv;
  asm volatile("s_mov_b32 %0, m0\n\ts_mov_b32 m0, %2\n\ts_nop 0\n\tglobal_load_lds_dword %1, off\n\ts_mov_b32 m0, %0" : "=&s"(sv) : "v"(p), "s"(lds_dst) : "memory");
}
DI void touch_code(const char* base, const char* end, int first, int lane, unsigned lds_dst) {
  const long n = (end - base) >> 7;
  const long l = first + lane;
  touch_to(base + (n > 0 ? (l < n ? l : n - 1) : 0) * 128, lds_dst);
}

#define K1_STORE(p, v) st16_sc1((p), (v))
constexpr int P1_SLOT = 32768, P1_T = 4 * P1_SLOT, P1_TROW = 1088, P1_SCR = P1_T + 8 * P1_TROW, P1_LDS = P1_SCR + 256 + 64;
struct Stage1 {
  unsigned vx0, vx1, vw0, vw1;
  const char *xt, *wt;
};
DI Stage1 stage1_addr(const bf16* x, const bf16* wqkv, int tb, int hq, int wv, int lane) {
  Stage1 a;
  const int row8 = lane >> 3, c8 = lane & 7, ra = 16 * wv + row8, rb = ra + 8;
  a.vx0 = ra * 1024 + ((c8 ^ ((ra >> 1) & 7)) << 4); a.vx1 = rb * 1024 + ((c8 ^ ((rb >> 1) & 7)) << 4);
  const unsigned sw = (c8 ^ (((row8 >> 1) & 1) << 2)) << 4;
  a.vw0 = ra * 3072 + sw; a.vw1 = rb * 3072 + sw;
  a.xt = (const char*)(x + (size_t)tb * 64 * D); a.wt = (const char*)(wqkv + hq * 64);
  return a;
}
DI void stage1_request(const Stage1& a, unsigned lds0, int wv, int kc) {
  const unsigned dst = lds0 + (kc & 3) * P1_SLOT + wv * 2048;
  glds16(a.xt + kc * 128 + a.vx0, dst); glds16(a.xt + kc * 128 + a.vx1, dst + 1024);
#pragma unroll
  for (int p = 0; p < 3; ++p) {
    glds16(a.wt + (size_t)kc * 64 * 3072 + p * 1024 + a.vw0, dst + 8192 + p * 8192);
    glds16(a.wt + (size_t)kc * 64 * 3072 + p * 1024 + a.vw1, dst + 8192 + p * 8192 + 1024);
  }
}
struct Stage1Lds { u32x4 xlo, xhi; unsigned wlo, whi; };
DI Stage1Lds stage1_lds(unsigned lds0, int w, int lane) {
  Stage1Lds a;
  const int r = lane & 31, h = lane >> 5, tw = w & 1, fh = (w >> 1) & 1, panel = w >> 2;
  const int q4 = (lane & 15) >> 2, p4 = lane & 3, blk = (lane >> 4) & 1;
  const int xrow = 32 * tw + r, xf = (xrow >> 1) & 7;
#pragma unroll
  for (int ks = 0; ks < 4; ++ks) { a.xlo[ks] = lds0 + xrow * 128 + (((2 * ks + h) ^ xf) << 4); a.xhi[ks] = a.xlo[ks] + 65536; }
  a.wlo = lds0 + 8192 + panel * 8192 + (8 * h + q4) * 128 + ((fh ^ ((q4 >> 1) & 1)) * 64) + 32 * blk + 8 * p4; a.whi = a.wlo + 65536;
  return a;
}
#define P1_IN(a) "{v[72:75]}"(a.xlo), "{v[76:79]}"(a.xhi), "{v80}"(a.wlo), "{v81}"(a.whi)


template <bool ISQ>
DI void qk_role(char* lds, char* ws, const bf16* x, const bf16* wqkv, const bf16* gw, int b, int w, f32x16& acc, float& gate) {
  const int xg = b & 7, bi = b >> 3, tb = (xg >> 1) * 8 + (bi & 7), hq = (xg & 1) * 4 + (bi >> 3);
  const unsigned lds0 = (unsigned)(uintptr_t)lds;
  const int tok = tb * 64 + hq * 8 + w, lane = lane_id();
  u32x4 lr, lgw;
  asm volatile("global_load_dwordx4 %0, %2, off\n\tglobal_load_dwordx4 %1, %3, off"
               : "=&v"(lr), "=&v"(lgw) : "v"(x + (size_t)tok * D + lane * 8), "v"(gw + lane * 8) : "memory");
  const Stage1 s1 = stage1_addr(x, wqkv, tb, hq, w & 3, lane);
  stage1_request(s1, lds0, w & 3, ISQ ? 0 : 1);
  asm volatile("s_waitcnt vmcnt(8)" : "+v"(lr), "+v"(lgw)::"memory");
  float a[8], s0 = 0.f, g0 = 0.f;
#pragma unroll
  for (int i = 0; i < 4; ++i) {
    a[2 * i] = bflo(lr[i]); a[2 * i + 1] = bfhi(lr[i]);
    s0 += a[2 * i] + a[2 * i + 1];
    g0 += a[2 * i] * bflo(lgw[i]) + a[2 * i + 1] * bfhi(lgw[i]);
  }
  const float mean = wave_total(s0) * (1.f / D);
  g0 = wave_total(g0);
  float v0 = 0.f;
#pragma unroll
  for (int i = 0; i < 8; ++i) { a[i] -= mean; v0 += a[i] * a[i]; }
  const float rs = 1.f / sqrtf(wave_total(v0) * (1.f / D) + 1e-5f);
  u32x4 o;
#pragma unroll
  for (int i = 0; i < 4; ++i) o[i] = cvtpk(a[2 * i] * rs, a[2 * i + 1] * rs);
  *(__attribute__((address_space(3))) u32x4*)((lds_ptr)lds + P1_T + w * P1_TROW + lane * 16) = o;
  gate = 1.f / (1.f + __expf(-g0));
  st16_sc1(ws + WS_O + ((size_t)b * 8 + w) * 1024 + lane * 16, ((u32x4){SENTINEL, SENTINEL, SENTINEL, SENTINEL}));
  const Stage1Lds la = stage1_lds(lds0, w, lane);
  const char* sx = s1.xt + 3 * 128;
  const char* swp = s1.wt + (size_t)3 * 64 * 3072;
#define P1_REQ "{v82}"(s1.vx0), "{v83}"(s1.vx1), "{v84}"(s1.vw0), "{v85}"(s1.vw1), "{s62}"(lds0 + (w & 3) * 2048)
  if (ISQ) asm volatile(P1Q_ASM : "={v[40:55]}"(acc), "+{s[56:57]}"(sx), "+{s[58:59]}"(swp) : P1_IN(la), P1_REQ : P1_CLOB);
  else asm volatile(P1K_ASM : "={v[40:55]}"(acc), "+{s[56:57]}"(sx), "+{s[58:59]}"(swp) : P1_IN(la), P1_REQ : P1_CLOB);
#undef P1_REQ
}

extern "C" __global__ __launch_bounds__(768, 3) void k_qkv(const bf16* __restrict__ x, const bf16* __restrict__ wqkv, const bf16* __restrict__ gw,
                                                char* __restrict__ ws) {
  extern __shared__ __attribute__((aligned(16))) char lds[];
  const int w = rfl(threadIdx.x >> 6), b = blockIdx.x;
  const int xg = b & 7, bi = b >> 3, tb = (xg >> 1) * 8 + (bi & 7), hq = (xg & 1) * 4 + (bi >> 3);
  const unsigned lds0 = (unsigned)(uintptr_t)lds;
  if (w < 8) {
    const int tok = tb * 64 + hq * 8 + w;
    float gate;
    f32x16 acc;
    if (w < 4) qk_role<true>(lds, ws, x, wqkv, gw, b, w, acc, gate);
    else qk_role<false>(lds, ws, x, wqkv, gw, b, w, acc, gate);
    const int ln = lane_id();
    const int ttile = tb * 2 + (w & 1), hA = hq * 4 + ((w >> 1) & 1) * 2;
    const float sc = w < 4 ? 0.25f * 1.4426950408889634f : 1.f;
    char* p = ws + (w < 4 ? WS_Q : WS_K) + ((size_t)hA * 64 + ttile) * 1024 + ln * 16;
    K1_STORE(p, pack8(acc, 0, sc)); K1_STORE(p + 65536, pack8(acc, 8, sc));
    lds_cptr t = (lds_cptr)lds + P1_T + ((ln & 15) >> 2) * P1_TROW + (64 * w + 16 * (ln >> 4) + 4 * (ln & 3)) * 2;
    const u32x4 hf = __builtin_bit_cast(u32x4, cat8(vtr(t), vtr(t + 4 * P1_TROW)));
    const int kst = (tb * 64 + hq * 8) >> 4, hh = hq & 1;
    K1_STORE(ws + WS_H + ((size_t)(2 * w + (ln >> 5)) * 128 + kst) * 1024 + hh * 512 + (ln & 31) * 16, hf);
    if (ln == 0) ((float*)(ws + WS_G))[tok] = gate;
    if (w == 0 && ln < 4) ((unsigned*)(ws + WS_F2))[b * 4 + ln] = 0u;
  } else {
    const int wv = w - 8;
    f32x16 acc;
    {
      const int lane = lane_id();
      const Stage1 s1 = stage1_addr(x, wqkv, tb, hq, wv, lane);
      {
        const int l = wv * 60 + (lane < 60 ? lane : 59);
        const int c = l / 40, i = l % 40;
        const char* tp = i < 24 ? s1.wt + (size_t)((2 + c) * 64 + 8 * (bi & 7) + i / 3) * 3072 + (i % 3) * 1024
                                : s1.xt + (size_t)(16 * ((bi >> 3) & 3) + (i - 24)) * 1024 + (2 + c) * 128;
        touch_to(tp, lds0 + P1_SCR);
      }
      if (wv == 0) touch_code(code_qkv(), code_mid(), 8, lane, lds0 + P1_SCR);
      else touch_code(code_mid(), code_end(), (wv - 1) * 64, lane, lds0 + P1_SCR);
      const char* sx2 = s1.xt + 2 * 128;
      const char* sw2 = s1.wt + (size_t)2 * 64 * 3072;
      const Stage1Lds la = stage1_lds(lds0, w, lane);
      const char* sx = s1.xt + 3 * 128;
      const char* swp = s1.wt + (size_t)3 * 64 * 3072;
      asm volatile(P1V_ASM : "={v[40:55]}"(acc), "+{s[56:57]}"(sx), "+{s[58:59]}"(swp)
                   : P1_IN(la), "{v82}"(s1.vx0), "{v83}"(s1.vx1), "{v84}"(s1.vw0), "{v85}"(s1.vw1), "{s62}"(lds0 + wv * 2048),
                     "{s[64:65]}"(sx2), "{s[66:67]}"(sw2) : P1_CLOB);
    }
    const int ln = lane_id();
    const int ttile = tb * 2 + (wv & 1), hA = hq * 4 + (wv >> 1) * 2;
    char* vp = ws + WS_V + ((size_t)(hA + ((ln >> 4) & 1)) * 64 + ttile) * 1024 + (ln >> 5) * 256 + (ln & 15) * 16;
    K1_STORE(vp, pack8(acc, 0, 1.f)); K1_STORE(vp + 512, pack8(acc, 8, 1.f));
  }
}

DI void kv_request(unsigned lds0, const char* kg, const char* vg, int c0, int c1, int w, int lane) {
  const unsigned vo = lane * 16;
#pragma unroll
  for (int c = c0; c < c1; ++c) {
    const int go = c * 8192 + w * 2048;
    unsigned sv;
    if (c == 0 && w == 0)
      asm volatile("s_mov_b32 %0, m0\n\ts_mov_b32 m0, %4\n\ts_nop 0\n\tglobal_load_lds_dwordx4 %1, %2 offset:1024\n\t"
                   "s_add_u32 m0, %4, 0x10000\n\ts_nop 0\n\tglobal_load_lds_dwordx4 %1, %3\n\tglobal_load_lds_dwordx4 %1, %3 offset:1024\n\ts_mov_b32 m0, %0"
                   : "=&s"(sv) : "v"(vo), "s"(kg + go), "s"(vg + go), "s"(lds0 + go) : "memory", "scc");
    else
      asm volatile("s_mov_b32 %0, m0\n\ts_mov_b32 m0, %4\n\ts_nop 0\n\tglobal_load_lds_dwordx4 %1, %2\n\tglobal_load_lds_dwordx4 %1, %2 offset:1024\n\t"
                   "s_add_u32 m0, %4, 0x10000\n\ts_nop 0\n\tglobal_load_lds_dwordx4 %1, %3\n\tglobal_load_lds_dwordx4 %1, %3 offset:1024\n\ts_mov_b32 m0, %0"
                   : "=&s"(sv) : "v"(vo), "s"(kg + go), "s"(vg + go), "s"(lds0 + go) : "memory", "scc");
  }
}
DI u32x4 no_sentinel(u32x4 v) {
#pragma unroll
  for (int i = 0; i < 4; ++i) v[i] = v[i] == SENTINEL ? 0x7fff7fffu : v[i];
  return v;
}
DI float max3(float a, float b, float c) { return __builtin_fmaxf(__builtin_fmaxf(a, b), c); }

DI void attn_waves(char* lds, char* ws, const bf16* w1, const bf16* w2, const bf16* wo, int b, int w) {
  const int head = (b & 7) * 4 + ((b >> 3) & 3), qt0 = (b >> 5) * 8 + w * 2;
  int lane = lane_id();
  const int r = lane & 31, h = lane >> 5;
  const unsigned lds0 = (unsigned)(uintptr_t)lds;
  const char* kg = ws + WS_K + (size_t)head * 65536;
  const char* vg = ws + WS_V + (size_t)head * 65536;
  __builtin_amdgcn_s_setprio(3);
  bf16x8 qA, qB, kf0;
  asm volatile("global_load_dwordx4 %0, %3, off\n\tglobal_load_dwordx4 %1, %3, off offset:1024\n\tglobal_load_dwordx4 %2, %4, off"
               : "=&v"(qA), "=&v"(qB), "=&v"(kf0) : "v"(ws + WS_Q + ((size_t)head * 64 + qt0) * 1024 + lane * 16), "v"(kg + lane * 16) : "memory");
  kv_request(lds0, kg, vg, 0, 2, w, lane);
  {
    const unsigned l1 = (b * 4 + w) * 64 + lane;
    touch((const char*)w1 + (size_t)(l1 < S * FF * 2 / 128 ? l1 : 0u) * 128, lds0);
  }
  asm volatile("s_waitcnt vmcnt(5)" : "+v"(qA), "+v"(qB), "+v"(kf0)::"memory");
  lds_arrive(lds, 0);
  f32x16 nm;
  {
    const f32x16 zero = {};
    const f32x16 sa = MFMA(kf0, qA, zero), sb = MFMA(kf0, qB, zero);
    float m = __builtin_fmaxf(sa[0], sb[0]);
#pragma unroll
    for (int i = 1; i < 16; ++i) m = max3(m, sa[i], sb[i]);
    m = -half_max(m);
#pragma unroll
    for (int i = 0; i < 16; ++i) nm[i] = m;
  }
  lds_spin(lds, 0, 4);
  f32x16 oa, ob;
  unsigned acnt = lds0 + P2_CNT + 4, asig = lds0 + P2_CNT + 16, ak = lds0 + lane * 16, av = lds0 + P2_V + h * 256 + (r & 15) * 16;
  const unsigned one = r == 16 ? 0x3f803f80u : 0u;
  const unsigned tl = (b * 4 + w) * 64 + lane;
  constexpr unsigned N2 = FF * S * 2 / 128, N3 = N2 + D * D * 2 / 128;
  const char* tch = tl < N2 ? (const char*)w2 + (size_t)tl * 128 : (const char*)wo + (size_t)((tl < N3 ? tl : N2) - N2) * 128;
  asm volatile(ATTN_ASM
               : "={v[96:111]}"(oa), "={v[112:127]}"(ob), "+{v182}"(acnt), "+{v185}"(asig), "+{v180}"(ak), "+{v181}"(av), "+{v[168:171]}"(kf0)
               : "{v[128:143]}"(nm), "{v[160:163]}"(qA), "{v[164:167]}"(qB), "{v184}"(one), "{v[186:187]}"(tch), "{s47}"(lds0 + P2_SCR)
               : ATTN_CLOB);
  lane = lane_id();
  oa[8] = row_sum(oa[8]); ob[8] = row_sum(ob[8]);
  if (!__all(range_ok(oa) && range_ok(ob))) {
    oa = attn_exact(kg, vg, qA, lane);
    ob = attn_exact(kg, vg, qB, lane);
  }
  char* op = ws + WS_O + ((size_t)qt0 * 32 + head) * 1024 + lane * 16;
  st16_sc1(op, no_sentinel(pack8(oa, 0, __builtin_amdgcn_rcpf(oa[8]))));
  st16_sc1(op + 32768, no_sentinel(pack8(ob, 0, __builtin_amdgcn_rcpf(ob[8]))));
  lds_arrive(lds, 7);
}

DI void st16(lds_ptr p, const f32x16& a) {
#pragma unroll
  for (int g = 0; g < 4; ++g) *(lds_f32x4*)(p + g * 1024) = (f32x4){a[4 * g], a[4 * g + 1], a[4 * g + 2], a[4 * g + 3]};
}
DI void add16(f32x16& s, lds_cptr p) {
#pragma unroll
  for (int g = 0; g < 4; ++g) {
    const f32x4 v = *(const lds_f32x4*)(p + g * 1024);
    s[4 * g] += v[0]; s[4 * g + 1] += v[1]; s[4 * g + 2] += v[2]; s[4 * g + 3] += v[3];
  }
}
DI void group_sync(char* lds, unsigned& nsync) { lds_arrive(lds, 16); lds_spin(lds, 16, 4 * (++nsync)); }
DI int dead_region(int hw) { return hw < 2 ? P2_V + hw * 16384 : 1024 + (hw - 2) * 16384; }
#define GEMM_IO(a0, a1, a2, a3) "={v[64:79]}"(a0), "={v[80:95]}"(a1), "={v[96:111]}"(a2), "={v[112:127]}"(a3)
#define GEMM_ADDR(ga) "{v176}"(ga.aw0), "{v177}"(ga.aw1), "{v178}"(ga.af), "{v179}"(ga.at0), "{v180}"(ga.at1), "{s62}"(ga.ring)
#define GEMM_PRE(NAME, wp, ga) asm volatile(NAME : "+{s[56:57]}"(wp) : "{v176}"(ga.aw0), "{v177}"(ga.aw1), "{s62}"(ga.ring) : "s63", "memory", "scc")
struct GemmAddr { unsigned aw0, aw1, af, at0, at1, ring; };
DI GemmAddr gemm_addr(unsigned ring, int lane, int ldw, bool perm, int wh) {
  GemmAddr a;
  const int row8 = lane >> 3, h = lane >> 5, q4 = (lane & 15) >> 2, p4 = lane & 3, blk = (lane >> 4) & 1;
  a.aw0 = row8 * ldw + (((lane & 7) ^ (((row8 >> 1) & 1) << 2)) << 4);
  a.aw1 = a.aw0 + 8 * ldw - 1024;
  a.af = lane * 16;
  const unsigned base = ring + ((perm ? 4 : 8) * h + q4) * 128 + 32 * blk + 8 * p4;
  a.at0 = base + ((((q4 >> 1) & 1) ^ wh) * 64);
  a.at1 = base + ((((q4 >> 1) & 1) ^ wh ^ 1) * 64);
  a.ring = ring;
  return a;
}
DI f32x2 gelu_tanh2(f32x2 x) {
  constexpr float c1 = -2.f * 0.7978845608028654f * 1.4426950408889634f, c2 = c1 * 0.044715f;
  const f32x2 z = x * __builtin_elementwise_fma(x * x, (f32x2){c2, c2}, (f32x2){c1, c1});
  const f32x2 d = (f32x2){__builtin_amdgcn_exp2f(z[0]), __builtin_amdgcn_exp2f(z[1])} + 1.f;
  return x * (f32x2){__builtin_amdgcn_rcpf(d[0]), __builtin_amdgcn_rcpf(d[1])};
}

DI void gemm_waves(char* lds, char* ws, const bf16* w1, const bf16* b1, const bf16* w2, const bf16* b2, const bf16* wo, bf16* out, int b, int hw) {
  const int xg = b & 7, bi = b >> 3, dq = (xg & 1) * 4 + (bi & 3), sb = (xg >> 1) * 8 + (bi >> 2);
  const unsigned lds0 = (unsigned)(uintptr_t)lds, ring = lds0 + P2_RING + hw * P2_RS;
  const int mh = hw >> 1, nh = hw & 1;
  int lane = lane_id();
  unsigned nsync = 0;
  const int head = (b & 7) * 4 + ((b >> 3) & 3);
  const char* kg = ws + WS_K + (size_t)head * 65536;
  const char* vg = ws + WS_V + (size_t)head * 65536;
  __builtin_amdgcn_s_setprio(0);
  if (hw < 3) touch_code(code_mid(), code_end(), 8 + hw * 64, lane, lds0 + P2_SCR);
  if (bi < 28) {
    const int fb = (xg >> 1) * 7 + (bi >> 2);
    const GemmAddr g1 = gemm_addr(ring, lane, FF * 2, false, mh);
    const char* wp1 = (const char*)(w1 + (size_t)hw * 512 * FF + fb * 64);
    GEMM_PRE(G1_PRE, wp1, g1);
    kv_request(lds0, kg, vg, 2, 8, hw, lane);
    const char* fq = ws + WS_H + ((size_t)(2 * dq) * 128 + hw * 32) * 1024;
    const char* f0 = fq + nh * (128 * 1024);
    const char* f1 = fq + (nh ^ 1) * (128 * 1024);
    u32x2 bv[4];
    const bf16* bp = b1 + fb * 64 + 32 * mh + 4 * (lane >> 5);
#pragma unroll
    for (int g = 0; g < 4; ++g) bv[g] = *(const u32x2*)(bp + 8 * g);
    f32x16 a0, a1, a2, a3;
    __builtin_amdgcn_s_setprio(0);
    asm volatile(G1_ASM
                 : GEMM_IO(a0, a1, a2, a3), "+{s[56:57]}"(wp1), "+{s[58:59]}"(f0), "+{s[60:61]}"(f1)
                 : GEMM_ADDR(g1), "{v181}"(lds0 + P2_CNT + 8)
                 : GEMM_CLOB);
    __builtin_amdgcn_s_setprio(3);
    lane = lane_id();
    lds_wait(lds, 4, 4);
    lds_ptr rb = (lds_ptr)lds + lane * 16;
    st16(rb + P2_RING + hw * P2_RS, a1); st16(rb + P2_RING + hw * P2_RS + 4096, a2); st16(rb + P2_V + hw * 4096, a3);
    group_sync(lds, nsync);
    add16(a0, rb + P2_RING + (hw ^ 1) * P2_RS);
    add16(a0, rb + P2_RING + (hw ^ 2) * P2_RS + 4096);
    add16(a0, rb + P2_V + (hw ^ 3) * 4096);
    f32x16 s = a0;
    asm volatile("" : "+v"(s));
#pragma unroll
    for (int g = 0; g < 4; ++g) {
      const f32x2 v0 = gelu_tanh2((f32x2){s[4 * g], s[4 * g + 1]} + (f32x2){bflo(bv[g][0]), bfhi(bv[g][0])});
      const f32x2 v1 = gelu_tanh2((f32x2){s[4 * g + 2], s[4 * g + 3]} + (f32x2){bflo(bv[g][1]), bfhi(bv[g][1])});
      s[4 * g] = v0[0]; s[4 * g + 1] = v0[1]; s[4 * g + 2] = v1[0]; s[4 * g + 3] = v1[1];
    }
    char* up = ws + WS_U + ((size_t)(2 * dq + nh) * 112 + fb * 4 + 2 * mh) * 1024 + lane * 16;
    st16_sc1(up, pack8(s, 0, 1.f)); st16_sc1(up + 1024, pack8(s, 8, 1.f));
    asm volatile("s_waitcnt vmcnt(0)" ::: "memory");
    if (lane == 0) st4_sc1((unsigned*)(ws + WS_F2) + (dq * 28 + fb) * 4 + hw, 1u);
    group_sync(lds, nsync);
  } else {
    kv_request(lds0, kg, vg, 2, 8, hw, lane);
    asm volatile("s_waitcnt vmcnt(0)" ::: "memory");
    lds_arrive(lds, 2); lds_arrive(lds, 3);
  }
  f32x16 y;
  float gate, bias2;
  {
    lane = lane_id();
    const GemmAddr ga = gemm_addr(ring, lane, S * 2, true, nh);
    const char* wp = (const char*)(w2 + (size_t)hw * 448 * S + sb * 64);
    const char* fq = ws + WS_U + ((size_t)(2 * dq) * 112 + hw * 28) * 1024;
    const char* f0 = fq + mh * (112 * 1024);
    const char* f1 = fq + (mh ^ 1) * (112 * 1024);
    GEMM_PRE(G2_PRE, wp, ga);
    {
      const int s = sb * 64 + 32 * nh + (lane & 31);
      gate = ((const float*)(ws + WS_G))[s];
      bias2 = __bfloat162float(b2[s]);
      asm volatile("" : "+v"(gate), "+v"(bias2));
    }
    poll_words((const unsigned*)(ws + WS_F2) + (dq * 28 + 7 * hw) * 4 + (lane < 28 ? lane : 0), true, 1u);
    {
      lds_wait(lds, 5, 4);
      const GemmAddr gq = gemm_addr(lds0 + dead_region(hw), lane, D * 2, true, 0);
      const char* w3 = (const char*)(wo + (size_t)hw * 128 * D + dq * 64);
      asm volatile(G3_PRE : "+{s[56:57]}"(w3) : "{v224}"(gq.aw0), "{v225}"(gq.aw1), "{s62}"(gq.ring) : "s63", "memory", "scc");
    }
    f32x16 a0, a1, a2, a3;
    __builtin_amdgcn_s_setprio(0);
    asm volatile(G2_ASM
                 : GEMM_IO(a0, a1, a2, a3), "+{s[56:57]}"(wp), "+{s[58:59]}"(f0), "+{s[60:61]}"(f1)
                 : GEMM_ADDR(ga)
                 : GEMM_CLOB);
    __builtin_amdgcn_s_setprio(3);
    lane = lane_id();
    lds_wait(lds, 6, 4);
    lds_ptr rb = (lds_ptr)lds + lane * 16;
    st16(rb + P2_RING + hw * P2_RS, a1); st16(rb + P2_RING + hw * P2_RS + 4096, a2); st16(rb + P2_V + 32768 + hw * 4096, a3);
    group_sync(lds, nsync);
    add16(a0, rb + P2_RING + (hw ^ 1) * P2_RS);
    add16(a0, rb + P2_RING + (hw ^ 2) * P2_RS + 4096);
    add16(a0, rb + P2_V + 32768 + (hw ^ 3) * 4096);
    y = a0;
#pragma unroll
    for (int i = 0; i < 16; ++i) y[i] = (1.f - gate) * (y[i] + bias2);
    asm volatile("s_waitcnt vmcnt(0)" : "+v"(y)::"memory");
    group_sync(lds, nsync);
  }
  {
    lane = lane_id();
    const GemmAddr g0 = gemm_addr(lds0 + dead_region(2 * mh), lane, D * 2, true, mh);
    const GemmAddr g1 = gemm_addr(lds0 + dead_region(2 * mh + 1), lane, D * 2, true, mh);
    const char* f0 = ws + WS_O + ((size_t)(2 * sb + nh) * 32 + 16 * mh) * 1024;
    lds_wait(lds, 7, 4);
    f32x16 a0, a1;
    asm volatile(G3_ASM
                 : "={v[64:79]}"(a0), "={v[80:95]}"(a1)
                 : "{s[58:59]}"(f0), "{v226}"(g0.af), "{v227}"((lane & 15) * 1024 + 1020),
                   "{v230}"(g0.at0), "{v231}"(g0.at1), "{v232}"(g1.at0), "{v233}"(g1.at1)
                 : G3_CLOB);
    lane = lane_id();
    lds_ptr rb = (lds_ptr)lds + P2_RING + lane * 16;
    st16(rb + hw * P2_RS, a1);
    lds_arrive(lds, 8 + hw);
    lds_spin(lds, 8 + (hw ^ 2), 1);
    add16(a0, rb + (hw ^ 2) * P2_RS);
    const f32x16 a = a0;
    unsigned p[8];
#pragma unroll
    for (int i = 0; i < 8; ++i) p[i] = cvtpk(gate * a[2 * i] + y[2 * i], gate * a[2 * i + 1] + y[2 * i + 1]);
    bf16* op = out + (size_t)(sb * 64 + 32 * nh + (lane & 31)) * D + dq * 64 + 32 * mh + (lane >> 5) * 8;
#pragma unroll
    for (int k = 0; k < 2; ++k) {
      const auto r0 = __builtin_amdgcn_permlane32_swap(p[4 * k], p[4 * k + 2], false, false);
      const auto r1 = __builtin_amdgcn_permlane32_swap(p[4 * k + 1], p[4 * k + 3], false, false);
      *(u32x4*)(op + 16 * k) = (u32x4){r0[0], r1[0], r0[1], r1[1]};
    }
  }
}

extern "C" __global__ __launch_bounds__(512, 2) void k_mid(const bf16* __restrict__ wo, const bf16* __restrict__ w1, const bf16* __restrict__ b1,
                                                const bf16* __restrict__ w2, const bf16* __restrict__ b2, char* __restrict__ ws, bf16* __restrict__ out) {
  extern __shared__ __attribute__((aligned(16))) char lds[];
  const int w = rfl(threadIdx.x >> 6);
  if (threadIdx.x < 32) ((lds_u32*)((lds_ptr)lds + P2_CNT))[threadIdx.x] = 0u;
  __syncthreads();
  if (w < 4) attn_waves(lds, ws, w1, w2, wo, blockIdx.x, w);
  else gemm_waves(lds, ws, w1, b1, w2, b2, wo, out, blockIdx.x, w - 4);
}

extern "C" __device__ __attribute__((used, noinline)) void k_end() {}

extern "C" void kernel_launch(void* const* d_in, const int* in_sizes, int n_in, void* d_out, int out_size, void* d_ws, size_t ws_size, hipStream_t stream) {
  const bf16* x = (const bf16*)d_in[0];
  const bf16* wqkv = (const bf16*)d_in[1];
  const bf16* wo = (const bf16*)d_in[2];
  const bf16* w1 = (const bf16*)d_in[4];
  const bf16* b1 = (const bf16*)d_in[5];
  const bf16* w2 = (const bf16*)d_in[6];
  const bf16* b2 = (const bf16*)d_in[7];
  const bf16* gw = (const bf16*)d_in[8];
  char* ws = (char*)d_ws;
  (void)hipFuncSetAttribute((const void*)k_qkv, hipFuncAttributeMaxDynamicSharedMemorySize, P1_LDS);
  (void)hipFuncSetAttribute((const void*)k_mid, hipFuncAttributeMaxDynamicSharedMemorySize, P2_LDS);
  k_qkv<<<256, 768, P1_LDS, stream>>>(x, wqkv, gw, ws);
  k_mid<<<256, 512, P2_LDS, stream>>>(wo, w1, b1, w2, b2, ws, (bf16*)d_out);
}
```
